```python
import torch

def _softmax(x, axis=-1):
    return torch.softmax(x, dim=axis)
def _gelu(x):
    return torch.nn.functional.gelu(x, approximate="tanh")
def _silu(x):
    return torch.nn.functional.silu(x)
def _sigmoid(x):
    return torch.sigmoid(x)
def _layernorm(x, axis=-1, eps=1e-5):
    m = x.mean(dim=axis, keepdim=True)
    v = x.var(dim=axis, keepdim=True, unbiased=False)
    return (x - m) / torch.sqrt(v + eps)
def _scatter_add(tgt, idx, src, axis=0):
    return tgt.clone().index_add_(axis, idx, src)
def _segment_sum(src, seg, n):
    out = torch.zeros((n,) + src.shape[1:], dtype=src.dtype)
    return out.index_add_(0, seg, src)
def _pad(x, pw):
    flat = tuple(v for pair in reversed(pw) for v in pair)
    return torch.nn.functional.pad(x, flat)

kernel_name = 'gated_mix__sliding_local_dilated__sliding_local_dilated__prompt'

def setup_inputs():

    _w = (lambda a: (lambda t: t.to(torch.bfloat16) if t.is_floating_point() else t)(torch.as_tensor(a)))
    def _A_setup():
        B, S, D = 1, 2048, 640
        return {
            "x": _w((lambda *s: torch.randn(*s, dtype=torch.bfloat16) * 0.05)(B, S, D)),
            "wqkv": _w((lambda *s: torch.randn(*s, dtype=torch.bfloat16) * 0.8)(D, 3 * D)),
            "wo": _w((lambda *s: torch.randn(*s, dtype=torch.bfloat16) * 0.05)(D, D)),
        }
    _A_in = _A_setup()

    def _B_setup():
        B, S, D = 1, 2048, 640
        return {
            "x": _w((lambda *s: torch.randn(*s, dtype=torch.bfloat16) * 0.05)(B, S, D)),
            "wqkv": _w((lambda *s: torch.randn(*s, dtype=torch.bfloat16) * 0.8)(D, 3 * D)),
            "wo": _w((lambda *s: torch.randn(*s, dtype=torch.bfloat16) * 0.05)(D, D)),
        }
    _B_in = _B_setup()

    return {
        "A_x": _A_in["x"],
        "A_wqkv": _A_in["wqkv"],
        "A_wo": _A_in["wo"],
        "B_x": _B_in["x"],
        "B_wqkv": _B_in["wqkv"],
        "B_wo": _B_in["wo"],
        "gate_w": _w((lambda *s: torch.randn(*s, dtype=torch.bfloat16) * 0.05)(640) * 16.0),
    }

def reference(A_x, A_wqkv, A_wo, B_x, B_wqkv, B_wo, gate_w):
    def _A_ref(x, wqkv, wo):
        B, S, D = x.shape
        H = 32; Dh = D // H
        W, dil = 8, 2
        qkv = (lambda x, *d: x.permute(*d))((x @ wqkv).reshape(B, S, 3, H, Dh), 2, 0, 3, 1, 4)
        q, k, v = qkv[0], qkv[1], qkv[2]
        scale = Dh ** -0.5
        pos = torch.arange(S)
        off = torch.arange(W) * dil
        idx = pos[:, None] - off[None, :]
        mask = idx >= 0
        idx_c = torch.where(mask, idx, 0).reshape(-1)
        kg = k[:, :, idx_c, :].reshape(B, H, S, W, Dh)
        vg = v[:, :, idx_c, :].reshape(B, H, S, W, Dh)
        logits = torch.einsum("bhsd,bhswd->bhsw", q, kg) * scale
        logits = torch.where(mask[None, None, :, :], logits, -1e9)
        attn = _softmax(logits, axis=-1)
        o = torch.einsum("bhsw,bhswd->bhsd", attn, vg)
        return (lambda x, *d: x.permute(*d))(o, 0, 2, 1, 3).reshape(B, S, D) @ wo

    def _B_ref(x, wqkv, wo):
        B, S, D = x.shape
        H = 32; Dh = D // H
        W, dil = 8, 2
        qkv = (lambda x, *d: x.permute(*d))((x @ wqkv).reshape(B, S, 3, H, Dh), 2, 0, 3, 1, 4)
        q, k, v = qkv[0], qkv[1], qkv[2]
        scale = Dh ** -0.5
        pos = torch.arange(S)
        off = torch.arange(W) * dil
        idx = pos[:, None] - off[None, :]
        mask = idx >= 0
        idx_c = torch.where(mask, idx, 0).reshape(-1)
        kg = k[:, :, idx_c, :].reshape(B, H, S, W, Dh)
        vg = v[:, :, idx_c, :].reshape(B, H, S, W, Dh)
        logits = torch.einsum("bhsd,bhswd->bhsw", q, kg) * scale
        logits = torch.where(mask[None, None, :, :], logits, -1e9)
        attn = _softmax(logits, axis=-1)
        o = torch.einsum("bhsw,bhswd->bhsd", attn, vg)
        return (lambda x, *d: x.permute(*d))(o, 0, 2, 1, 3).reshape(B, S, D) @ wo

    x = A_x
    g = _sigmoid((x * gate_w).sum(-1, keepdims=True))
    a = _A_ref(x, A_wqkv, A_wo)
    b = _B_ref(x, B_wqkv, B_wo)
    return g * a + (1 - g) * b
```

```cpp
#include <hip/hip_runtime.h>
#include <hip/hip_bf16.h>
typedef __hip_bfloat16 bf16;
constexpr int S=2048, D=640, H=32, DH=20;

__device__ inline float b2f(bf16 v){ return __bfloat162float(v); }
__device__ inline float rb(float v){ return __bfloat162float(__float2bfloat16(v)); }

__global__ void k_qkv(const bf16* x, const bf16* wa, const bf16* wb, float* qkv){
  int n = blockIdx.x*256+threadIdx.x;
  int s0 = blockIdx.y*8;
  int br = n/1920, c = n%1920;
  const bf16* w = br? wb: wa;
  float acc[8]={0,0,0,0,0,0,0,0};
  for(int k=0;k<D;k++){ float wv=b2f(w[k*1920+c]);
    #pragma unroll
    for(int i=0;i<8;i++) acc[i]+=b2f(x[(s0+i)*D+k])*wv; }
  #pragma unroll
  for(int i=0;i<8;i++) qkv[((size_t)br*S+s0+i)*1920+c]=rb(acc[i]);
}
__global__ void k_attn(const float* qkv, float* o){
  int idx = blockIdx.x*256+threadIdx.x;
  int h = idx%H, s=(idx/H)%S, br=idx/(H*S);
  const float* base = qkv+(size_t)br*S*1920;
  float lg[8]; float m=-1e30f;
  #pragma unroll
  for(int w=0;w<8;w++){ int t=s-2*w; float a=0; int tt=t<0?0:t;
    for(int d=0;d<DH;d++) a+=base[(size_t)s*1920+h*DH+d]*base[(size_t)tt*1920+640+h*DH+d];
    a=rb(rb(a)*0.22360679774997896f); if(t<0)a=-1e9f; lg[w]=a; m=fmaxf(m,a);}
  float sum=0;
  #pragma unroll
  for(int w=0;w<8;w++){ lg[w]=expf(lg[w]-m); sum+=lg[w]; }
  #pragma unroll
  for(int w=0;w<8;w++) lg[w]=rb(lg[w]/sum);
  for(int d=0;d<DH;d++){ float a=0;
    #pragma unroll
    for(int w=0;w<8;w++){ int t=s-2*w; int tt=t<0?0:t; a+=lg[w]*base[(size_t)tt*1920+1280+h*DH+d]; }
    o[((size_t)br*S+s)*D+h*DH+d]=rb(a);}
}
__global__ void k_out(const bf16* x,const bf16* gw,const float* o,const bf16* woa,const bf16* wob,bf16* out){
  int s=blockIdx.x;
  __shared__ float red[640];
  int n=threadIdx.x;
  red[n]=rb(b2f(x[s*D+n])*b2f(gw[n]));
  __syncthreads();
  float gs=0; for(int k=0;k<D;k++) gs+=red[k];
  gs=rb(gs); float g=rb(1.f/(1.f+expf(-gs)));
  float a=0,b=0;
  for(int k=0;k<D;k++){ a+=o[(size_t)s*D+k]*b2f(woa[k*D+n]); b+=o[((size_t)S+s)*D+k]*b2f(wob[k*D+n]); }
  a=rb(a); b=rb(b);
  out[s*D+n]=__float2bfloat16(rb(g*a)+rb(rb(1.f-g)*b));
}
extern "C" void kernel_launch(void* const* d_in, const int* in_sizes, int n_in,
                              void* d_out, int out_size, void* d_ws, size_t ws_size,
                              hipStream_t stream) {
  const bf16* x=(const bf16*)d_in[0]; const bf16* wa=(const bf16*)d_in[1]; const bf16* woa=(const bf16*)d_in[2];
  const bf16* wb=(const bf16*)d_in[4]; const bf16* wob=(const bf16*)d_in[5]; const bf16* gw=(const bf16*)d_in[6];
  float* qkv=(float*)d_ws; float* o=qkv+(size_t)2*S*1920;
  k_qkv<<<dim3(15,S/8),256,0,stream>>>(x,wa,wb,qkv);
  k_attn<<<2*S*H/256,256,0,stream>>>(qkv,o);
  k_out<<<S,640,0,stream>>>(x,gw,o,woa,wob,(bf16*)d_out);
}
```

```cpp
#define RT0 0
#define DELAY0 4
#include <hip/hip_runtime.h>
#include <hip/hip_bf16.h>
#include <utility>

typedef __bf16 bf16x8 __attribute__((ext_vector_type(8)));
typedef __bf16 bf16x4 __attribute__((ext_vector_type(4)));
typedef __bf16 bf16x2 __attribute__((ext_vector_type(2)));
typedef float f32x4 __attribute__((ext_vector_type(4)));
typedef float f32x2 __attribute__((ext_vector_type(2)));
typedef unsigned u32x6 __attribute__((ext_vector_type(6)));
typedef unsigned u32x4 __attribute__((ext_vector_type(4)));
typedef unsigned u32x2 __attribute__((ext_vector_type(2)));
typedef unsigned long long u64;
#define LAS __attribute__((address_space(3)))
#define GAS __attribute__((address_space(1)))
#define DI __device__ __forceinline__
#define AI __attribute__((always_inline))
#define SB() __builtin_amdgcn_sched_barrier(0)
#define PIN(x) asm volatile("" : "+v"(x))
#define RLX __ATOMIC_RELAXED, __HIP_MEMORY_SCOPE_AGENT

constexpr int STG = 49152;
constexpr int XOFF = 30720;
constexpr int EXCH = 143360;
constexpr int GWL = EXCH + 3 * 5120;
constexpr int FLAG_L = XOFF;
constexpr int RT_L = EXCH + 5120;
constexpr int LDS_BYTES = 163840;
constexpr int WS_E = 0;
constexpr int WS_FLAG = 2048;
constexpr int WS_GATE = 4096;
constexpr int WS_O = 32768;
constexpr int REC = 5376;
constexpr int O_BYTES = 16 * 16 * 4 * REC;
constexpr int WS_W = WS_O + O_BYTES;
constexpr int SPIN = 1 << 16;
#define OA0 17
#define OA1 17
#define OS 17
#define ES 1
#define UNP 1
#define LATE0 1
#define BPOS 12
#define LV 1
#define NUC 12
#define CSTEP 1
#define NUP 12
#define CODE_BYTES 28672
#define NINI 24
#define STAMP(i)

DI void bar() { asm volatile("s_barrier" ::: "memory"); }
DI void bar_l() { __builtin_amdgcn_s_waitcnt(0xc07f); asm volatile("s_barrier" ::: "memory"); }
template <int N> DI void bar_v() { asm volatile("s_waitcnt vmcnt(%0)\n\ts_barrier" :: "i"(N) : "memory"); }
DI bf16x8 cat(bf16x4 a, bf16x4 b) { return __builtin_shufflevector(a, b, 0, 1, 2, 3, 4, 5, 6, 7); }
DI bf16x8 cat(u32x2 a, u32x2 b) { u32x4 r = {a[0], a[1], b[0], b[1]}; return __builtin_bit_cast(bf16x8, r); }
DI u32x6 cat3(u32x2 a, u32x2 b, u32x2 c) { u32x6 r = {a[0], a[1], b[0], b[1], c[0], c[1]}; return r; }
DI bf16x8 lo4(u32x6 t) { return __builtin_bit_cast(bf16x8, __builtin_shufflevector(t, t, 0, 1, 2, 3)); }
DI bf16x8 hi4(u32x6 t) { return __builtin_bit_cast(bf16x8, __builtin_shufflevector(t, t, 2, 3, 4, 5)); }
DI bf16x4 trd(LAS char* p) { return __builtin_amdgcn_ds_read_tr16_b64_v4bf16((LAS bf16x4*)p); }
DI unsigned pk2(float a, float b) { f32x2 v = {a, b}; return __builtin_bit_cast(unsigned, __builtin_convertvector(v, bf16x2)); }
DI u32x2 pk4(f32x4 v) { u32x2 r = {pk2(v[0], v[1]), pk2(v[2], v[3])}; return r; }
DI f32x4 mfma(bf16x8 a, bf16x8 b, f32x4 c) { return __builtin_amdgcn_mfma_f32_16x16x32_bf16(a, b, c, 0, 0, 0); }
DI float rbf(float v) { return __builtin_bit_cast(float, pk2(v, v) << 16); }
DI u64 mktag(u64 e) { return e * 6364136223846793005ull + 1442695040888963407ull; }
DI unsigned sum4(u32x4 v) { return v[0] + v[1] + v[2] + v[3]; }
DI float fmx(float a, float b) { return __builtin_elementwise_maximum(a, b); }
DI float fbits(unsigned u) { return __builtin_bit_cast(float, u); }
DI unsigned ubits(float f) { return __builtin_bit_cast(unsigned, f); }
template <class F, int... I> DI void sfor_(F&& f, std::integer_sequence<int, I...>) { (f(std::integral_constant<int, I>{}), ...); }
template <int N, class F> DI void sfor(F&& f) { sfor_(f, std::make_integer_sequence<int, N>{}); }
#define IC(v) std::integral_constant<int, (v)>{}

#define PFIT -1
#define NIT 10
#define CARRY_ 4
#define PRE_ 1
#define EGAP_ 5
constexpr int CARRY = CARRY_;
constexpr int PRE = PRE_;
constexpr int EGAP = EGAP_;
constexpr int GATE_F = 4;
constexpr bool has_halo(int W, int f) { return f >= 5 && ((f - 5) & 3) == W; }
constexpr int grp_n(int W, int n) { return (has_halo(W, n % 15) || (W == 2 && n % 15 == GATE_F)) ? 3 : 2; }
constexpr int m_grp(int W, int m) { int n = 30 - CARRY; while (m >= grp_n(W, n % 30)) { m -= grp_n(W, n % 30); ++n; } return n - (30 - CARRY); }
constexpr int m_idx(int W, int m) { int n = 30 - CARRY; while (m >= grp_n(W, n % 30)) { m -= grp_n(W, n % 30); ++n; } return m; }
constexpr int n_mfma(int W) { int t = 0; for (int n = 0; n < 30; ++n) t += grp_n(W, n); return t; }
constexpr int n_read(int W) { return W == 2 ? 68 : 66; }
constexpr int rd_code(int W, int j) {
  constexpr int pos[2][10] = {{0, 3, 10, -1, -1, 29, 32, 41, -1, -1}, {0, 3, 8, -1, 11, 30, 33, 40, -1, 43}};
  int k = 0;
  for (int i = 0; i < 10; ++i) { const int p = pos[W == 2][i]; if (p < 0) continue; if (j == p) return 60 + i; if (j > p) ++k; }
  return j - k;
}

template <int W>
DI void gemm1(LAS char* lds, int lane, int hg, const char* pfb, unsigned pfo, f32x4 (&acc)[2][15], float& logit) {
  const int g = lane >> 4, i16 = lane & 15, q = i16 >> 2, p = lane & 3;
  const unsigned rowb = (16 * (g >> 1) + 4 * (g & 1) + q) * 480;
  const unsigned aM = rowb + 8 * p, aX = rowb + 40 * p + 32;
  const unsigned aA = XOFF + i16 * 128 + ((g ^ (i16 >> 1)) * 16);
  const int rh = i16 < 8 ? 16 + 8 * hg + i16 : i16;
  const unsigned aH = XOFF + rh * 128 + ((g ^ ((rh >> 1) & 7)) * 16);
  f32x4 hal[3];
#pragma unroll
  for (int t = 0; t < 2; ++t)
#pragma unroll
    for (int n = 0; n < 15; ++n) acc[t][n] = f32x4{0, 0, 0, 0};
#pragma unroll
  for (int n = 0; n < 3; ++n) hal[n] = f32x4{0, 0, 0, 0};
  bf16x8 cw[CARRY], cx[3];
  const bf16x8 z8 = __builtin_bit_cast(bf16x8, u32x4{0, 0, 0, 0});
#pragma unroll
  for (int n = 0; n < CARRY; ++n) cw[n] = z8;
#pragma unroll
  for (int n = 0; n < 3; ++n) cx[n] = z8;
  auto one = [&](int f, int i, bf16x8 wf, bf16x8 xf) AI {
    f32x4& d = i < 2 ? acc[i][f] : hal[(f - 5) >> 2];
    d = f < 10 ? mfma(wf, xf, d) : mfma(xf, wf, d);
  };
  constexpr int NM = n_mfma(W), NR = n_read(W), NP = W == 2 ? 7 : 6;
  unsigned buf = 0;
  LAS char* pp[7] = {lds + aM, lds + aX, lds + aA, lds + (aA ^ 64), lds + aH, lds + (aH ^ 64), lds + (GWL + g * 16)};
  int it = NIT, dl = STG;
#pragma unroll 1
  do {
    bf16x4 wh[60]; bf16x8 xf[10];
    auto rd = [&](auto J) AI {
      constexpr int c = rd_code(W, J.value);
      if constexpr (c < 60) {
        constexpr int n = c >> 1, ks = n / 15, f = n % 15, sec = f / 5, jj = f % 5;
        wh[c] = trd((jj < 4 ? pp[0] + 40 * jj : pp[1]) + (ks * 32 + (c & 1) * 8) * 480 + sec * 160);
      } else {
        constexpr int ks = (c - 60) / 5, t = (c - 60) % 5;
        xf[c - 60] = *(LAS bf16x8*)(t < 2 ? pp[2 + ks] + (2 * W + 1 + t) * 2048 : t == 2 ? pp[4 + ks] : pp[6] + ks * 64);
      }
    };
    sfor<NM>([&](auto M) AI {
      constexpr int m = M.value, gi = m_grp(W, m), i = m_idx(W, m);
      if constexpr (m == PRE) { bar_l(); SB(); }
      if constexpr (gi < CARRY) one(15 - CARRY + gi, i, cw[gi], cx[i]);
      else {
        constexpr int n = gi - CARRY, ks = n / 15;
        if constexpr (W == 2 && n % 15 == GATE_F && i == 2) hal[2] = mfma(xf[ks * 5 + 4], xf[ks * 5 + 2], hal[2]);
        else one(n % 15, i, cat(wh[2 * n], wh[2 * n + 1]), xf[ks * 5 + i]);
      }
      SB();
      constexpr int D2 = NR - (NM - EGAP), j = m - PRE;
      if constexpr (j >= 0 && j < D2) { rd(IC(2 * j)); rd(IC(2 * j + 1)); }
      else if constexpr (j >= 0 && D2 + j < NR) rd(IC(D2 + j));
      else if constexpr (j >= 0) {
        constexpr int u = m - (NM - (EGAP - PRE)), NG = EGAP - PRE, PER = (NP + NG - 1) / NG;
        sfor<PER>([&](auto K) AI { constexpr int k = u * PER + K.value; if constexpr (k < NP) { pp[k] += (k == 6 ? 128 : dl); PIN(pp[k]); } });
      }
      if constexpr (W == 3 && PFIT >= 0 && m == NM - 2) {
        if (it == PFIT) asm volatile("s_mov_b32 m0, %2\n\ts_nop 0\n\tglobal_load_lds_dword %0, %1" :: "v"(pfo), "s"(pfb), "s"((unsigned)(size_t)lds + XOFF + 512) : "memory");
      }
      if constexpr (m == 21) { dl = (buf == 2 * STG) ? -2 * STG : STG; asm volatile("" : "+s"(dl)); }
      if constexpr (m == 23) { buf += dl; --it; asm volatile("" : "+s"(buf), "+s"(it)); }
      SB();
    });
#pragma unroll
    for (int n = 0; n < CARRY; ++n) cw[n] = cat(wh[2 * (30 - CARRY + n)], wh[2 * (30 - CARRY + n) + 1]);
#pragma unroll
    for (int n = 0; n < 3; ++n) cx[n] = xf[5 + n];
  } while (it);
#pragma unroll
  for (int gi = 0; gi < CARRY; ++gi)
#pragma unroll
    for (int i = 0; i < 3; ++i)
      if (i < grp_n(W, 30 - CARRY + gi)) one(15 - CARRY + gi, i, cw[gi], cx[i]);
#pragma unroll
  for (int f = 5; f < 15; ++f)
    if (has_halo(W, f)) *(LAS u32x2*)(lds + EXCH + (f - 5) * 512 + lane * 8) = pk4(hal[(f - 5) >> 2]);
  if (W == 2) logit = hal[2][0];
}

struct AttnConst { f32x4 biasC, biasP, biasP0; unsigned selC[2], selP[2], gm[4]; };
DI bf16x8 q_operand(int h, u32x2 qh, u32x2 qx, unsigned gm) { const u32x2 qm = {qx[0] & gm, qx[1] & gm}; return (h & 1) ? cat(qm, qh) : cat(qh, qm); }
DI bf16x8 k_operand(int h, const u32x6 (&k)[2]) { return (h & 1) ? hi4(k[h >> 1]) : lo4(k[h >> 1]); }
#define PACE 3
#define STR2(x) #x
#define STR(x) STR2(x)
#define PW "s_waitcnt vmcnt(" STR(PACE) ")\n\t"
#define DMA1(v, srd, so, mo, si, mi) "s_add_u32 m0, " mo ", " #mi "\n\ts_add_u32 %0, " so ", " #si "\n\tbuffer_load_dwordx4 " v ", " srd ", %0 offen sc1 lds\n\t"
#define DMA(v, srd, so, mo, si, mi) "s_add_u32 m0, " mo ", " #mi "\n\ts_add_u32 %0, " so ", " #si "\n\tbuffer_load_dwordx4 " v ", " srd ", %0 offen lds\n\t"

typedef unsigned u32x16 __attribute__((ext_vector_type(16)));
DI const char* ptr_of(u32x16 k, int i) { return (const char*)(((u64)k[2 * i + 1] << 32) | k[2 * i]); }
DI __amdgpu_buffer_rsrc_t rsrc(const char* p, int bytes) { return __builtin_amdgcn_make_buffer_rsrc((void*)p, 0, bytes, 0x00020000); }

extern "C" __global__ __launch_bounds__(512, 2) void fused_kernel(const char*, const char*, const char*, const char*, const char*, const char*, char*, char*) {
  extern __shared__ __attribute__((aligned(16))) char smem[];
  u32x16 ka;
  asm volatile("s_load_dwordx16 %0, %1, 0x0" : "=&s"(ka) : "s"(__builtin_amdgcn_kernarg_segment_ptr()));
  LAS char* lds = (LAS char*)smem;
  const unsigned ldsb = (unsigned)(size_t)lds;
  const int tid = threadIdx.x, lane = tid & 63;
  const int wid = __builtin_amdgcn_readfirstlane(tid >> 6);
  const int b = blockIdx.x, xcd = b & 7, r = b >> 3;
  const int tg = (xcd & 1) * 8 + (r & 7), hg = (xcd >> 1) * 4 + (r >> 3);
  const int par = tg >> 3, ch = tg & 7;
  const int kq = wid & 3, th = wid >> 2;
#define CNT(i) (void)0
  STAMP(0);
  if (wid == 2) {
    const char* pc; unsigned back; const unsigned len = CODE_BYTES;
    asm volatile("s_getpc_b64 %0\nfk_here:\n\ts_mov_b32 %1, fk_here-fused_kernel" : "=s"(pc), "=s"(back));
    pc -= back;
    const unsigned po = (r * 8 + (lane & 7)) * 128;
    if (lane < 8 && po < len) asm volatile("s_mov_b32 m0, %2\n\ts_nop 0\n\tglobal_load_lds_dword %0, %1" :: "v"(po), "s"(pc), "s"(ldsb + XOFF) : "memory");
  }
  const int stg0 = 2 * ((ch + hg) % 5) + ((par + (hg >> 2)) & 1);
  const int l30 = lane >= 30 ? lane - 30 : lane, sec = (l30 >= 10) + (l30 >= 20);
  unsigned vW = (lane >= 30 ? 3840 : 0) + sec * (1280 - 160) + l30 * 16;
  unsigned vX0 = (lane >> 3) * 2560 + (((lane & 7) ^ (lane >> 4)) * 16), vX1 = vX0 ^ 64;
  unsigned vH = (lane >> 3) * 2560 + (((lane & 7) ^ (4 + kq)) * 16);
  unsigned oW = kq * 16 * 3840 + (hg & 7) * 160;
  unsigned oX = (2 * (128 * ch + 32 * kq) + par) * 1280;
  unsigned oH = (2 * (128 * ch + 2 * kq - (ch ? 8 : 0)) + par) * 1280;
  unsigned sW0 = oW + stg0 * (64 * 3840), sX0 = oX + stg0 * 128, sH0 = oH + stg0 * 128;
  unsigned dW = ldsb + kq * 7680, dX = ldsb + XOFF + (16 + 32 * kq) * 128, dH = ldsb + XOFF + (8 + 2 * kq) * 128;
  asm volatile("s_waitcnt lgkmcnt(0)" : "+s"(ka), "+v"(vW), "+v"(vX0), "+v"(vX1), "+v"(vH), "+s"(sW0), "+s"(sX0), "+s"(sH0), "+s"(dW), "+s"(dX), "+s"(dH));
  const char* const x = ptr_of(ka, 0);
  char* const ws = (char*)ptr_of(ka, 7);

  const auto wsrd = rsrc(hg >= 8 ? ptr_of(ka, 2) : ptr_of(ka, 1), 640 * 3840), xsrd = rsrc(x, 2048 * 1280);
  auto partA = [&](unsigned sw_, unsigned dw) AI {
    unsigned t;
    asm volatile(
      "s_mov_b32 exec_hi, 0x0fffffff\n\t"
      DMA("%1", "%2", "%3", "%4", 0, 0) DMA("%1", "%2", "%3", "%4", 7680, 960) DMA("%1", "%2", "%3", "%4", 30720, 1920)
      DMA("%1", "%2", "%3", "%4", 38400, 2880) DMA("%1", "%2", "%3", "%4", 15360, 3840) DMA("%1", "%2", "%3", "%4", 23040, 4800)
      "s_mov_b32 exec_hi, -1"
      : "=&s"(t) : "v"(vW), "s"(wsrd), "s"(sw_), "s"(dw) : "scc", "memory");
  };
  auto partB = [&](unsigned sw_, unsigned sx, unsigned sh, unsigned dw, unsigned dx, unsigned dh) AI {
    unsigned t;
    asm volatile(
      DMA("%2", "%6", "%8", "%11", 0, 0) DMA("%3", "%6", "%8", "%11", 20480, 1024) DMA("%2", "%6", "%8", "%11", 40960, 2048) DMA("%3", "%6", "%8", "%11", 61440, 3072)
      "s_mov_b32 exec_hi, 0x0fffffff\n\t"
      DMA("%1", "%5", "%7", "%10", 46080, 5760) DMA("%1", "%5", "%7", "%10", 53760, 6720)
      "s_mov_b64 exec, 0xffff\n\t"
      DMA("%4", "%6", "%9", "%12", 0, 0)
      "s_mov_b64 exec, -1"
      : "=&s"(t)
      : "v"(vW), "v"(vX0), "v"(vX1), "v"(vH), "s"(wsrd), "s"(xsrd), "s"(sw_), "s"(sx), "s"(sh), "s"(dw), "s"(dx), "s"(dh)
      : "scc", "memory");
  };
  if (wid >= 4) {
    STAMP(14);
    partB(sW0, sX0, sH0, dW, dX, dH); partA(sW0, dW);
    if (kq < 2) {
      const int c = kq * 64 + lane, sg = stg0 + (c >> 3);
      unsigned t;
      asm volatile(DMA("%1", "%2", "0", "%3", 0, 0) : "=&s"(t)
                   : "v"(c < 80 ? (sg >= 10 ? sg - 10 : sg) * 128 + (c & 7) * 16 : 2048), "s"(rsrc(ptr_of(ka, 3), 1280)), "s"(ldsb + GWL + kq * 1024) : "scc", "memory");
    }
    partA(stg0 == 9 ? oW : sW0 + 64 * 3840, dW + STG);
    STAMP(9);
  }
  SB();
  u64 epoch;
  const int g = lane >> 4, i16 = lane & 15;
  const int hf = (hg >> 2) & 1, ct = (hg & 3) + 4 * (hg >> 3);
  unsigned so[4][3];
  LAS char *pw, *pw3;
  auto setup3 = [&]() AI {
    int tq = tg; asm volatile("" : "+s"(tq));
#pragma unroll
    for (int i = 0; i < 4; ++i) {
      const unsigned ob = ((tq * 16 + 4 * kq + ((tq + i) & 3)) * 4 + 2 * hf + th) * REC;
      so[i][0] = ob + (tq & 4) * 256; so[i][1] = ob + ((tq & 4) ^ 4) * 256; so[i][2] = ob + 4096;
      asm volatile("" : "+s"(so[i][0]), "+s"(so[i][1]), "+s"(so[i][2]));
    }
  };
  unsigned oo[3], go[3]; LAS char* pwr;
  auto setup4 = [&]() AI {
    int td = tid; PIN(td);
#pragma unroll
    for (int e = 0; e < 3; ++e) {
      const int c = e == 1 ? 256 + (td & 63) : td < 320 ? td : 0, tk = c / 10, tok = tk + (e == 2 ? 32 : 0), j = c - tk * 10;
      oo[e] = (2 * (128 * ch + 64 * hf + tok) + par) * 1280 + ct * 160 + j * 16; PIN(oo[e]);
      go[e] = WS_GATE + (tg * 128 + 64 * hf + tok) * 8; PIN(go[e]);
    }
    pwr = lds + ((th ? 0 : 122880) + kq * 10240 + ((td >> 4) & 1) * 5120 + (td & 15) * 160 + ((td >> 5) & 1) * 16);
    PIN(pwr);
  };
  bf16x8 pre[4][5];
  auto pre_read = [&]() AI {
    int ln = lane; PIN(ln);
    pw = lds + ((ln >> 2) * 160 + 8 * (ln & 3) + kq * 5120); pw3 = pw + 3 * 20480;
    PIN(pw); PIN(pw3);
#pragma unroll
    for (int ps = 0; ps < 4; ++ps)
#pragma unroll
      for (int c5 = 0; c5 < 5; ++c5) pre[ps][c5] = cat(trd(pw3 + ps * 20480 + c5 * 32), trd(pw3 + ps * 20480 + c5 * 32 + 2560));
  };
  u32x4 late[3][5];

  u64 gv[3];
  auto project = [&](auto THC) AI {
    constexpr bool TH = THC.value;
    STAMP(4);
    const u64 tag = mktag(epoch);
    const auto orsrc = rsrc(ws + WS_O, O_BYTES);
    struct Rec { u32x4 m[2][2], x; unsigned cs; };
    Rec rec[4];
    const unsigned l16 = lane * 16, l4 = lane * 4;
    auto ldrec = [&](int i, int j) AI {
      if (j < 4) rec[i].m[j >> 1][j & 1] = __builtin_amdgcn_raw_buffer_load_b128(orsrc, l16 + (j & 1) * 2048, so[i][j >> 1], 16);
      else if (j == 4) rec[i].x = __builtin_amdgcn_raw_buffer_load_b128(orsrc, l16, so[i][2], 16);
      else rec[i].cs = __builtin_amdgcn_raw_buffer_load_b32(orsrc, l4 + 1024, so[i][2], 16);
    };
    auto reload = [&](int i) AI {
      Rec& q = rec[i];
      asm volatile("buffer_load_dwordx4 %0, %6, %8, %9 offen sc1\n\tbuffer_load_dwordx4 %1, %6, %8, %9 offen offset:2048 sc1\n\t"
                   "buffer_load_dwordx4 %2, %6, %8, %10 offen sc1\n\tbuffer_load_dwordx4 %3, %6, %8, %10 offen offset:2048 sc1\n\t"
                   "buffer_load_dwordx4 %4, %6, %8, %11 offen sc1\n\tbuffer_load_dword %5, %7, %8, %11 offen offset:1024 sc1\n\ts_waitcnt vmcnt(0)"
                   : "+v"(q.m[0][0]), "+v"(q.m[0][1]), "+v"(q.m[1][0]), "+v"(q.m[1][1]), "+v"(q.x), "+v"(q.cs)
                   : "v"(l16), "v"(l4), "s"(orsrc), "s"(so[i][0]), "s"(so[i][1]), "s"(so[i][2]) : "memory");
    };
    auto word = [&](int i, int k) AI { return k < 16 ? rec[i].m[k >> 3][(k >> 2) & 1][k & 3] : rec[i].x[k & 3]; };
    auto stale = [&](int i) AI {
      unsigned c = (unsigned)tag;
#pragma unroll
      for (int k = 0; k < 20; ++k) c += word(i, k);
      return __builtin_amdgcn_ballot_w64(c != rec[i].cs);
    };
    auto refetch = [&](int i, int n) AI {
#pragma unroll 1
      for (int spin = 0; spin < SPIN; ++spin) { CNT(i); reload(i); if (!stale(i)) break; }
#pragma unroll
      for (int k = 1; k < n; ++k) reload(i + k);
    };
    __builtin_amdgcn_s_setprio(TH ? 1 : 2);
    if constexpr (TH) {
      LAS char* lp = lds + 3 * 20480 + kq * 5120 + lane * 16;
#pragma unroll
      for (int ps = 0; ps < 3; ++ps)
#pragma unroll
        for (int i = 0; i < 5; ++i) *(LAS u32x4*)(lp + ((ps + 2) % 3) * 20480 + i * 1024) = late[(ps + 2) % 3][i];
      SB(); setup3(); setup4();
      bar_l();
    }
    SB();
    constexpr int NU = TH ? NUP : NUC, ST = TH ? 1 : CSTEP;
    sfor<NU>([&](auto J) AI { ldrec(J.value / 6, J.value % 6); SB(); });
    STAMP(6);
    SB();
    bf16x4 wq[10][10];
    auto rdq = [&](auto P, auto I) AI {
      constexpr int G[10] = {3, 4, 5, 6, 0, 1, 2, 3, 4, 5};
      wq[P.value][I.value] = trd((G[P.value] < 3 ? pw + G[P.value] * 20480 : pw3 + (G[P.value] - 3) * 20480) + (I.value >> 1) * 32 + (I.value & 1) * 2560);
    };
    SB();
    if (__builtin_expect(stale(0) != 0, 0)) refetch(0, NU / 6);
    if constexpr (TH && LV) { if (__builtin_expect(stale(1) != 0, 0)) refetch(1, NU / 6 - 1); }
    STAMP(5);
    f32x4 a3[2][5];
    unsigned csum = 0; u64 bad = 0; bf16x8 xo[2];
    sfor<100>([&](auto M) AI {
      constexpr int m = M.value;
      constexpr int pos = m < 70 ? m / 10 : 7 + ((m - 70) % 6) / 2, c5 = m < 70 ? (m % 10) >> 1 : (m - 70) / 6, t = m & 1;
      constexpr int pp = pos < 4 ? pos : pos - 1;
      if constexpr ((m == 20 && !(TH && LV)) || m == 50 || m == 70) { constexpr int i = m == 20 ? 1 : m == 50 ? 2 : 3; if (__builtin_expect(bad != 0, 0)) refetch(i, i == 1 ? 1 : 4 - i); SB(); }
      if constexpr (m == 80 && TH) { bar(); SB(); }
      const bf16x8 ov = (pos == 4 || pos == 9) ? xo[t] : __builtin_bit_cast(bf16x8, rec[pp >> 1].m[pp & 1][t]);
      const bf16x8 wv = pos < 4 ? pre[pos][c5] : cat(wq[pos][2 * c5], wq[pos][2 * c5 + 1]);
      if constexpr (m < 10) asm volatile("v_mfma_f32_16x16x32_bf16 %0, %1, %2, 0" : "=&v"(a3[t][c5]) : "v"(wv), "v"(ov));
      else asm volatile("v_mfma_f32_16x16x32_bf16 %0, %1, %2, %0" : "+v"(a3[t][c5]) : "v"(wv), "v"(ov));
      SB();
      if constexpr (m % ST == 0 && m / ST < 24 - NU) ldrec((NU + m / ST) / 6, (NU + m / ST) % 6);
      if constexpr (!TH && m == BPOS) bar();
      if constexpr (!TH && m > (24 - NU) * ST && m <= (24 - NU) * ST + 3) gv[m - (24 - NU) * ST - 1] = __hip_atomic_load((const GAS u64*)(ws + go[m - (24 - NU) * ST - 1]), RLX);
      if constexpr (TH && m == 24 - NU) { gv[2] = __hip_atomic_load((const GAS u64*)(ws + go[2]), RLX); gv[0] = gv[1] = gv[2]; }
      if constexpr (m >= 20 && m < 50) rdq(IC(m / 10 + 2), IC(m % 10));
      if constexpr (m >= 50 && m < 80) rdq(IC(7 + ((m - 50) % 6) / 2), IC(2 * ((m - 50) / 6) + (m & 1)));
      constexpr int ci = m < 20 ? 1 : m < 50 ? 2 : 3, cb = m < 20 ? 8 : m < 50 ? 36 : 56, k = m - cb;
      if constexpr (m < 70 && (m >= 20 || !(TH && LV)) && k >= 0 && k < 10) { csum = (k ? csum : (unsigned)tag) + word(ci, 2 * k) + word(ci, 2 * k + 1); PIN(csum); }
      if constexpr (m < 70 && (m >= 20 || !(TH && LV)) && k == 10) { bad = __builtin_amdgcn_ballot_w64(csum != rec[ci].cs); asm volatile("" : "+s"(bad)); }
      if constexpr (m == 30 || m == 31 || m == 70 || m == 71) {
        constexpr int r0 = m < 70 ? 0 : 2;
        xo[t] = cat(u32x2{rec[r0].x[2 * t], rec[r0].x[2 * t + 1]}, u32x2{rec[r0 + 1].x[2 * t], rec[r0 + 1].x[2 * t + 1]}); PIN(xo[t]);
      }
      if constexpr (m >= 82 && (m - 82) % 6 < 2) *(LAS f32x4*)(pwr + t * 16 * 160 + ((m - 82) / 6) * 32) = a3[t][(m - 82) / 6];
      SB();
    });
    *(LAS f32x4*)(pwr + 3 * 32) = a3[0][3];
    asm volatile("s_nop 1" : "+v"(a3[1][3]));
    *(LAS f32x4*)(pwr + 16 * 160 + 3 * 32) = a3[1][3];
    asm volatile("s_nop 5" : "+v"(a3[0][4]), "+v"(a3[1][4]));
    *(LAS f32x4*)(pwr + 4 * 32) = a3[0][4];
    *(LAS f32x4*)(pwr + 16 * 160 + 4 * 32) = a3[1][4];
    STAMP(7);
  };

  if (__builtin_expect(wid >= 4, 0)) {
    int s1 = stg0 == 9 ? 0 : stg0 + 1, s2 = stg0 >= 8 ? stg0 - 8 : stg0 + 2;
    asm volatile("s_load_dwordx2 %0, %1, 0x0\n\ts_waitcnt lgkmcnt(0)" : "=&s"(epoch) : "s"(ws + WS_E) : "memory");
    unsigned rt_src[2], rt_dst[2];
#pragma unroll
    for (int i = 0; i < 2; ++i) {
      const int c = i * 64 + lane, cu = c / 40, rem = c - cu * 40, r4 = rem / 10, wi = rem - r4 * 10;
      const int u = 10 * (b & 127) + (kq < 2 ? 3 * kq : 2 + 2 * kq) + cu, row = (u >> 3) * 4, cq = u & 7;
      const int h8 = row / 80, hh = (row - h8 * 80) / 20, d = row - h8 * 80 - hh * 20;
      const bool on = cu < (kq < 2 ? 3 : 2);
      rt_src[i] = on ? (row + r4) * 1280 + cq * 160 + wi * 16 : 0x7ffff000u;
      rt_dst[i] = on ? (((b >> 7) * 8 + cq) * 8 + h8) * 12800 + ((d < 16 ? hh * 16 + d : 48 + hh * 4 + d) + r4) * 160 + wi * 16 : 0x7ffff000u;
    }
    const unsigned fo = WS_FLAG + (64 * kq + lane) * 4;
    const auto wosrd = rsrc(ws + WS_W + (kq >> 1) * 819200, 819200);
    const int rot = tg & 3, sw = (tg >> 2) & 1;
    const unsigned l16 = lane * 16;
    auto wo_off = [&](int pos, unsigned& v2, unsigned& sa, unsigned& sb) AI {
      const unsigned base = (ct * 8 + 4 * (kq & 1)) * 12800;
      if (pos == 4 || pos == 9) {
        sa = base + ((rot + (pos == 9 ? 2 : 0)) & 3) * 12800 + 10240; sb = base + ((rot + (pos == 9 ? 3 : 1)) & 3) * 12800 + 10240 - 2560;
        v2 = l16 + (lane >= 32 ? sb : sa);
      } else {
        const int pp = pos < 4 ? pos : pos - 1;
        sa = sb = base + ((rot + (pp >> 1)) & 3) * 12800 + ((pp & 1) ^ sw) * 5120;
        v2 = l16 + sa;
      }
    };
    auto wo_pos = [&](int pos, int grp) AI {
      unsigned v2, sa, sb, t;
      wo_off(pos, v2, sa, sb);
      asm volatile(DMA1("%1", "%3", "%4", "%6", 0, 0) DMA1("%1", "%3", "%4", "%6", 1024, 1024) DMA1("%2", "%3", "0", "%6", 2048, 2048) DMA1("%1", "%3", "%5", "%6", 3072, 3072) DMA1("%1", "%3", "%5", "%6", 4096, 4096)
                   : "=&s"(t) : "v"(l16), "v"(v2), "s"(wosrd), "s"(sa), "s"(sb), "s"(ldsb + grp * 20480 + kq * 5120) : "scc", "memory");
    };
    auto late_load = [&](int ps) AI {
      unsigned v2, sa, sb;
      wo_off(7 + ps, v2, sa, sb);
#pragma unroll
      for (int i = 0; i < 5; ++i) late[ps][i] = __builtin_amdgcn_raw_buffer_load_b128(wosrd, i == 2 ? v2 : l16, (i == 2 ? 0 : i < 2 ? sa : sb) + i * 1024, 16);
    };
    unsigned b1 = STG, b2 = 2 * STG;
    unsigned q0, q1, q2, q3, q4, q5, q6, q7;
    auto prep = [&]() AI {
      q0 = oW + s1 * (64 * 3840); q1 = oX + s1 * 128; q2 = oH + s1 * 128; q3 = dW + b1; q4 = dX + b1; q5 = dH + b1; q6 = oW + s2 * (64 * 3840); q7 = dW + b2;
      asm volatile("" : "+s"(q0), "+s"(q1), "+s"(q2), "+s"(q3), "+s"(q4), "+s"(q5), "+s"(q6), "+s"(q7));
      s1 = s2; s2 = (s2 == 9) ? 0 : s2 + 1;
      b1 = b2; b2 = (b2 == 2 * STG) ? 0 : b2 + STG;
    };
    const unsigned rtl = ldsb + RT_L + kq * 2048;
    auto rt_load = [&]() AI {
      asm volatile("s_mov_b32 m0, %3\n\ts_nop 0\n\tbuffer_load_dwordx4 %0, %2, 0 offen lds\n\ts_add_u32 m0, %3, 1024\n\ts_nop 0\n\tbuffer_load_dwordx4 %1, %2, 0 offen lds\n\ts_waitcnt vmcnt(8)"
                   :: "v"(rt_src[0]), "v"(rt_src[1]), "s"(rsrc(b >> 7 ? ptr_of(ka, 5) : ptr_of(ka, 4), 819200)), "s"(rtl) : "scc", "memory");
    };
    prep();
    if (RT0 < 0) rt_load(); else asm volatile("s_waitcnt vmcnt(6)" ::: "memory");
#pragma unroll 1
    for (int it = 0; it < 8; ++it) {
      SB(); bar(); SB();
      partB(q0, q1, q2, q3, q4, q5); partA(q6, q7);
      SB();
      prep();
      if (it == RT0) {
        rt_load();
      } else if (it == 6) {
        asm volatile("s_mov_b32 m0, %2\n\ts_nop 0\n\tglobal_load_lds_dword %0, %1 sc1\n\ts_waitcnt vmcnt(7)" :: "v"(fo), "s"(ws), "s"(ldsb + FLAG_L + kq * 256) : "memory");
      } else if (it == RT0 + 2) {
        const u32x4 rt0 = *(LAS u32x4*)(lds + RT_L + kq * 2048 + lane * 16), rt1 = *(LAS u32x4*)(lds + RT_L + 1024 + kq * 2048 + lane * 16);
        asm volatile("buffer_store_dwordx4 %0, %2, %4, 0 offen sc0 sc1\n\tbuffer_store_dwordx4 %1, %3, %4, 0 offen sc0 sc1\n\ts_waitcnt vmcnt(8)"
                     :: "v"(rt0), "v"(rt1), "v"(rt_dst[0]), "v"(rt_dst[1]), "s"(rsrc(ws + WS_W, 2 * 819200)) : "memory");
      } else {
        if (it == RT0 + 4 && tid == 256) __hip_atomic_store((GAS unsigned*)(ws + WS_FLAG) + b, (unsigned)mktag(epoch), __ATOMIC_RELAXED, __HIP_MEMORY_SCOPE_SYSTEM);
        asm volatile("s_waitcnt vmcnt(6)" ::: "memory");
      }
    }
    bar();
    STAMP(19);
    partB(q0, q1, q2, q3, q4, q5);
    if (__builtin_expect(__builtin_amdgcn_ballot_w64(*(LAS unsigned*)(lds + FLAG_L + kq * 256 + lane * 4) != (unsigned)mktag(epoch)) != 0, 0)) {
#pragma unroll 1
      for (int spin = 0; spin < SPIN; ++spin) if (CNT(3), __builtin_amdgcn_ballot_w64(__hip_atomic_load((const GAS unsigned*)(ws + fo), RLX) != (unsigned)mktag(epoch)) == 0) break;
      asm volatile("" ::: "memory");
    }
    wo_pos(0, 3);
    bar_v<5>();
    STAMP(13);
    wo_pos(1, 4); wo_pos(2, 5); wo_pos(3, 6);
    late_load(2); late_load(0);
    STAMP(1);
    bar();
    STAMP(2);
    wo_pos(4, 0); wo_pos(5, 1); wo_pos(6, 2);
    late_load(1);
    asm volatile("s_waitcnt vmcnt(30)" ::: "memory");
    pre_read();
    STAMP(3);
    bar();
    __builtin_amdgcn_s_waitcnt(0xc07f);
    bar_v<5>();
    project(IC(1));
  } else {
    asm volatile("s_load_dwordx2 %0, %1, 0x0" : "=&s"(epoch) : "s"(ws + WS_E) : "memory");
    const float NINF = -__builtin_inff();
    AttnConst ac;
    const bool first = (ch == 0) && (wid == 0);
#pragma unroll
    for (int j = 0; j < 4; ++j) {
      const int c = 4 * g + j;
      ac.biasC[j] = (c <= i16 && c >= i16 - 7) ? 0.f : NINF;
      ac.biasP[j] = (c >= i16 + 9) ? 0.f : NINF;
      ac.biasP0[j] = first ? NINF : ac.biasP[j];
      ac.gm[j] = (g == j) ? 0xffffffffu : 0u; PIN(ac.gm[j]);
    }
#pragma unroll
    for (int j = 0; j < 2; ++j) {
      const bool c0 = 4 * g + 2 * j <= i16, c1 = 4 * g + 2 * j + 1 <= i16;
      ac.selC[j] = (c0 ? 0x0302u : 0x0c0cu) | (c1 ? 0x07060000u : 0x0c0c0000u); PIN(ac.selC[j]);
      ac.selP[j] = (c0 ? 0x0c0cu : 0x0302u) | (c1 ? 0x0c0c0000u : 0x07060000u); PIN(ac.selP[j]);
    }
    PIN(ac.biasC); PIN(ac.biasP); PIN(ac.biasP0);
    setup3();
    if (wid == 1) {
      const unsigned o1 = (2 * (128 * ch + 64 * hf) + par) * 1280 + (lane & 1) * 126 * 1280;
      if (lane < 2) asm volatile("s_mov_b32 m0, %2\n\ts_nop 0\n\tglobal_load_lds_dword %0, %1" :: "v"(o1), "s"(ptr_of(ka, 6)), "s"(ldsb + XOFF + 256) : "memory");
    }
    const char* pfb = b >> 7 ? ptr_of(ka, 5) : ptr_of(ka, 4);
    unsigned pfo = (b & 127) * 6400 + (lane < 50 ? lane : 49) * 128; PIN(pfo);
    f32x4 acc[2][15]; float logit = 0.f;
    STAMP(9);
    if (wid == 0) gemm1<0>(lds, lane, hg, pfb, pfo, acc, logit);
    else if (wid == 1) gemm1<1>(lds, lane, hg, pfb, pfo, acc, logit);
    else if (wid == 2) gemm1<2>(lds, lane, hg, pfb, pfo, acc, logit);
    else gemm1<3>(lds, lane, hg, pfb, pfo, acc, logit);
    asm volatile("s_waitcnt vmcnt(0)" : "+s"(epoch) :: "memory");
    STAMP(1);
    const auto orsrc = rsrc(ws + WS_O, O_BYTES);
    u32x6 K[3][2], V[5];
    auto set2 = [&](u32x6& t, int i, u32x2 v) AI { t[i] = v[0]; t[i + 1] = v[1]; };
    K[2][0] = cat3(pk4(acc[1][5]), pk4(acc[1][9]), pk4(acc[1][6])); PIN(K[2][0]);
    K[2][1] = cat3(pk4(acc[1][7]), u32x2{K[2][0][2], K[2][0][3]}, pk4(acc[1][8])); PIN(K[2][1]);
#pragma unroll
    for (int f = 0; f < 5; ++f) { asm volatile("" : "=v"(V[f])); set2(V[f], 4, pk4(acc[1][10 + f])); PIN(V[f]); }
    if (wid < 3) {
      LAS char* e = lds + EXCH + (wid + 1) * 5120 + lane * 8;
      *(LAS u32x2*)(e) = u32x2{K[2][0][0], K[2][0][1]}; *(LAS u32x2*)(e + 512) = u32x2{K[2][0][4], K[2][0][5]};
      *(LAS u32x2*)(e + 1024) = u32x2{K[2][1][0], K[2][1][1]}; *(LAS u32x2*)(e + 1536) = u32x2{K[2][1][4], K[2][1][5]};
      *(LAS u32x2*)(e + 2048) = u32x2{K[2][0][2], K[2][0][3]};
#pragma unroll
      for (int f = 0; f < 5; ++f) *(LAS u32x2*)(e + (5 + f) * 512) = u32x2{V[f][4], V[f][5]};
    }
    SB();
    const u32x2 qx[2] = {pk4(acc[0][4]), pk4(acc[1][4])};
    bf16x8 qb[2][4]; f32x4 sc[2][4], sp[2][4];
    auto scores = [&](int p) AI {
      K[1][p] = cat3(pk4(acc[0][5 + 2 * p]), p ? u32x2{K[1][0][2], K[1][0][3]} : pk4(acc[0][9]), pk4(acc[0][6 + 2 * p])); PIN(K[1][p]);
#pragma unroll
      for (int t = 1; t >= 0; --t)
#pragma unroll
        for (int h = 2 * p; h < 2 * p + 2; ++h) {
          qb[t][h] = q_operand(h, pk4(acc[t][h]), qx[t], ac.gm[h]);
          sc[t][h] = mfma(k_operand(h, K[t + 1]), qb[t][h], ac.biasC);
          if (t) sp[t][h] = mfma(k_operand(h, K[t]), qb[t][h], ac.biasP);
        }
    };
    scores(0);
    SB();
    bar_l();
    STAMP(2);
    SB();
    {
      LAS char* e = lds + EXCH + wid * 5120 + lane * 8;
      K[0][0] = cat3(*(volatile LAS u32x2*)(e), *(volatile LAS u32x2*)(e + 2048), *(volatile LAS u32x2*)(e + 512));
      set2(V[0], 0, *(volatile LAS u32x2*)(e + 5 * 512)); set2(V[1], 0, *(volatile LAS u32x2*)(e + 6 * 512)); set2(V[4], 0, *(volatile LAS u32x2*)(e + 9 * 512));
      K[0][1] = cat3(*(volatile LAS u32x2*)(e + 1024), *(volatile LAS u32x2*)(e + 2048), *(volatile LAS u32x2*)(e + 1536));
      set2(V[2], 0, *(volatile LAS u32x2*)(e + 7 * 512)); set2(V[3], 0, *(volatile LAS u32x2*)(e + 8 * 512));
    }
    const unsigned obase = ((tg * 16 + hg) * 4 + wid) * REC;
    unsigned cs = (unsigned)mktag(epoch);
    auto put = [&](int k, u32x4 v) AI {
      __builtin_amdgcn_raw_buffer_store_b128(v, orsrc, lane * 16 + (k & 3) * 1024, obase + (k >> 2) * 4096, OS);
      cs += sum4(v);
    };
    const float CS = 0.22360679774997896f * 1.4426950408889634f;
    const bf16x8 ones = __builtin_bit_cast(bf16x8, u32x4{0x3f803f80u, 0x3f803f80u, 0x3f803f80u, 0x3f803f80u});
    const f32x4 z4 = {0, 0, 0, 0};
    f32x4 s[2][4], l[2][4], o[2][4], e[2][4]; float mc[2][4], rl[2][4]; u32x2 op[2][4];
    auto maxima = [&](int p) AI {
      unsigned ml[4];
#pragma unroll
      for (int h = 2 * p; h < 2 * p + 2; ++h) { if (h == 2 * p) PIN(K[0][p]); sp[0][h] = mfma(k_operand(h, K[0]), qb[0][h], ac.biasP0); }
#pragma unroll
      for (int u = 3; u >= 0; --u) {
        const int t = u >> 1, h = 2 * p + (u & 1);
        s[t][h] = __builtin_elementwise_maximum(sc[t][h], sp[t][h]);
        ml[u] = ubits(fmx(fmx(fmx(s[t][h][0], s[t][h][1]), s[t][h][2]), s[t][h][3]));
      }
      auto r2 = __builtin_amdgcn_permlane16_swap(ml[2], ml[3], false, false);
      auto r1 = __builtin_amdgcn_permlane16_swap(ml[0], ml[1], false, false);
      auto r3 = __builtin_amdgcn_permlane32_swap(ubits(fmx(fbits(r1[0]), fbits(r1[1]))), ubits(fmx(fbits(r2[0]), fbits(r2[1]))), false, false);
      const unsigned w = ubits(fmx(fbits(r3[0]), fbits(r3[1])) * -CS);
      auto r4 = __builtin_amdgcn_permlane32_swap(w, w, false, false);
      auto r5 = __builtin_amdgcn_permlane16_swap(r4[0], r4[0], false, false);
      auto r6 = __builtin_amdgcn_permlane16_swap(r4[1], r4[1], false, false);
      mc[0][2 * p] = fbits(r5[0]); mc[0][2 * p + 1] = fbits(r5[1]); mc[1][2 * p] = fbits(r6[0]); mc[1][2 * p + 1] = fbits(r6[1]);
    };
    auto weigh = [&](int h) AI {
#pragma unroll
      for (int t = 1; t >= 0; --t) {
        const f32x4 a = __builtin_elementwise_fma(s[t][h], f32x4{CS, CS, CS, CS}, f32x4{mc[t][h], mc[t][h], mc[t][h], mc[t][h]});
        unsigned pr[4];
#pragma unroll
        for (int j = 0; j < 4; ++j) pr[j] = ubits(__builtin_amdgcn_exp2f(a[j]));
        const bf16x8 pb = cat(u32x2{__builtin_amdgcn_perm(pr[1], pr[0], ac.selP[0]), __builtin_amdgcn_perm(pr[3], pr[2], ac.selP[1])},
                              u32x2{__builtin_amdgcn_perm(pr[1], pr[0], ac.selC[0]), __builtin_amdgcn_perm(pr[3], pr[2], ac.selC[1])});
        l[t][h] = mfma(ones, pb, z4);
        o[t][h] = mfma(t ? hi4(V[h]) : lo4(V[h]), pb, z4);
        e[t][h] = mfma(t ? hi4(V[4]) : lo4(V[4]), pb, z4);
      }
    };
    auto emit = [&](int p) AI {
#pragma unroll
      for (int t = 1; t >= 0; --t) {
#pragma unroll
        for (int h = 2 * p; h < 2 * p + 2; ++h) { rl[t][h] = __builtin_amdgcn_rcpf(l[t][h][0]); op[t][h] = pk4(o[t][h] * rl[t][h]); }
        put(2 * t + p, u32x4{op[t][2 * p][0], op[t][2 * p][1], op[t][2 * p + 1][0], op[t][2 * p + 1][1]});
      }
    };
    set2(V[0], 2, pk4(acc[0][10])); set2(V[1], 2, pk4(acc[0][11])); set2(V[4], 2, pk4(acc[0][14]));
    maxima(0);
    PIN(V[0]); PIN(V[1]); PIN(V[4]);
    weigh(0); weigh(1); SB();
    scores(1); emit(0); SB();
    set2(V[2], 2, pk4(acc[0][12])); set2(V[3], 2, pk4(acc[0][13]));
    maxima(1);
    PIN(V[2]); PIN(V[3]);
    u32x4 ex[2]; unsigned rx[2];
#pragma unroll
    for (int t = 1; t >= 0; --t) {
      ex[t] = (__builtin_bit_cast(u32x4, e[t][1]) & ac.gm[1]) | (__builtin_bit_cast(u32x4, e[t][0]) & ~ac.gm[1]);
      rx[t] = (ubits(rl[t][1]) & ac.gm[1]) | (ubits(rl[t][0]) & ~ac.gm[1]);
    }
    weigh(2); weigh(3); SB();
    if (wid == 2) {
      const float gt = rbf(__builtin_amdgcn_rcpf(1.f + __expf(-rbf(logit))));
      if (lane < 8)
        __hip_atomic_store((GAS u64*)(ws + WS_GATE + (tg * 128 + 8 * hg + lane) * 8), ((u64)(unsigned)mktag(epoch) << 32) | ubits(gt), __ATOMIC_RELAXED, OS == 17 ? __HIP_MEMORY_SCOPE_SYSTEM : __HIP_MEMORY_SCOPE_AGENT);
    }
    emit(1); SB();
    u32x2 ox[2];
#pragma unroll
    for (int t = 1; t >= 0; --t) {
#pragma unroll
      for (int h = 2; h < 4; ++h) { ex[t] = (__builtin_bit_cast(u32x4, e[t][h]) & ac.gm[h]) | (ex[t] & ~ac.gm[h]); rx[t] = (ubits(rl[t][h]) & ac.gm[h]) | (rx[t] & ~ac.gm[h]); }
      ox[t] = pk4(__builtin_bit_cast(f32x4, ex[t]) * fbits(rx[t]));
    }
    put(4, u32x4{ox[0][0], ox[0][1], ox[1][0], ox[1][1]});
    {
      const u32x4 c4 = {(unsigned)__builtin_amdgcn_mov_dpp((int)cs, 0x00, 0xf, 0xf, true), (unsigned)__builtin_amdgcn_mov_dpp((int)cs, 0x55, 0xf, 0xf, true),
                        (unsigned)__builtin_amdgcn_mov_dpp((int)cs, 0xaa, 0xf, 0xf, true), (unsigned)__builtin_amdgcn_mov_dpp((int)cs, 0xff, 0xf, 0xf, true)};
      if ((lane & 3) == 0) __builtin_amdgcn_raw_buffer_store_b128(c4, orsrc, lane * 4, obase + 5120, OS);
    }
    STAMP(3);
    setup4();
    bar();
    pre_read();
    STAMP(10);
    asm volatile("s_waitcnt vmcnt(0)" ::: "memory");
    for (int i = 0; i < DELAY0; ++i) __builtin_amdgcn_s_sleep(2);
    STAMP(11);
    bar_l();
    project(IC(0));
  }
  const u64 tag = mktag(epoch);
  if (__builtin_expect(__builtin_amdgcn_ballot_w64(((unsigned)(gv[0] >> 32) ^ (unsigned)tag) | ((unsigned)(gv[1] >> 32) ^ (unsigned)tag) | ((unsigned)(gv[2] >> 32) ^ (unsigned)tag)) != 0, 0)) {
#pragma unroll
    for (int e = 0; e < 3; ++e)
#pragma unroll 1
      for (int spin = 0; spin < SPIN; ++spin) { gv[e] = __hip_atomic_load((const GAS u64*)(ws + go[e]), RLX); if (__builtin_amdgcn_ballot_w64((unsigned)(gv[e] >> 32) != (unsigned)tag) == 0) break; }
  }
  char* const out = (char*)ptr_of(ka, 6);
  auto finish = [&](auto E, LAS char* pr) AI {
    constexpr int e = E.value;
    const float gt = fbits((unsigned)gv[e]), gn = rbf(1.f - gt);
    const f32x4 lo = (*(LAS f32x4*)(pr) + *(LAS f32x4*)(pr + 10240)) * gt + (*(LAS f32x4*)(pr + 20480) + *(LAS f32x4*)(pr + 30720)) * gn;
    const f32x4 hi = (*(LAS f32x4*)(pr + 5120) + *(LAS f32x4*)(pr + 15360)) * gt + (*(LAS f32x4*)(pr + 25600) + *(LAS f32x4*)(pr + 35840)) * gn;
    const u32x2 l2 = pk4(lo), h2 = pk4(hi);
    if ((e < 2 ? OA0 : OA1) < 0) *(GAS u32x4*)(out + oo[e]) = u32x4{l2[0], l2[1], h2[0], h2[1]};
    else __builtin_amdgcn_raw_buffer_store_b128(u32x4{l2[0], l2[1], h2[0], h2[1]}, rsrc(out, 2048 * 1280), oo[e], 0, e < 2 ? (OA0 < 0 ? 0 : OA0) : (OA1 < 0 ? 0 : OA1));
  };
  if (!th) {
    __builtin_amdgcn_s_setprio(0);
    bar_l();
    finish(IC(0), lds + 122880 + tid * 16);
    if (wid == 0) finish(IC(1), lds + 122880 + (256 + lane) * 16);
  }
  bar_l();
  if (wid < 5) finish(IC(2), lds + tid * 16);
  if (b == 0 && tid == 0) { if (ES) __hip_atomic_store((GAS u64*)(ws + WS_E), tag, __ATOMIC_RELAXED, __HIP_MEMORY_SCOPE_SYSTEM); else *(GAS u64*)(ws + WS_E) = tag; }
  STAMP(8);
}

extern "C" void kernel_launch(void* const* d_in, const int* in_sizes, int n_in, void* d_out, int out_size, void* d_ws, size_t ws_size, hipStream_t stream) {
  (void)in_sizes; (void)n_in; (void)out_size; (void)ws_size;
  (void)hipFuncSetAttribute((const void*)fused_kernel, hipFuncAttributeMaxDynamicSharedMemorySize, LDS_BYTES);
  fused_kernel<<<256, 512, LDS_BYTES, stream>>>((const char*)d_in[0], (const char*)d_in[1], (const char*)d_in[4], (const char*)d_in[6], (const char*)d_in[2],
                                                (const char*)d_in[5], (char*)d_out, (char*)d_ws);
}
```

```cpp
#include <hip/hip_runtime.h>
#include <hip/hip_bf16.h>
#include <utility>

typedef __bf16 bf16x8 __attribute__((ext_vector_type(8)));
typedef __bf16 bf16x4 __attribute__((ext_vector_type(4)));
typedef __bf16 bf16x2 __attribute__((ext_vector_type(2)));
typedef float f32x4 __attribute__((ext_vector_type(4)));
typedef float f32x2 __attribute__((ext_vector_type(2)));
typedef unsigned u32x6 __attribute__((ext_vector_type(6)));
typedef unsigned u32x4 __attribute__((ext_vector_type(4)));
typedef unsigned u32x2 __attribute__((ext_vector_type(2)));
typedef unsigned long long u64;
#define LAS __attribute__((address_space(3)))
#define GAS __attribute__((address_space(1)))
#define DI __device__ __forceinline__
#define AI __attribute__((always_inline))
#define SB() __builtin_amdgcn_sched_barrier(0)
#define PIN(x) asm volatile("" : "+v"(x))
#define RLX __ATOMIC_RELAXED, __HIP_MEMORY_SCOPE_AGENT

constexpr int STG = 49152;
constexpr int XOFF = 30720;
constexpr int EXCH = 143360;
constexpr int GWL = EXCH + 3 * 5120;
constexpr int LDS_BYTES = 163840;
constexpr int WS_E = 0;
constexpr int WS_GATE = 4096;
constexpr int WS_O = 32768;
constexpr int REC = 5376;
constexpr int O_BYTES = 16 * 16 * 4 * REC;
constexpr int SPIN = 1 << 16;
#define OA0 17
#define OA1 17
#define OS 17
#define ES 1
#define UNP 1
#define LATE0 1
#define BPOS 12
#define LV 1
#define NUC 12
#define CSTEP 1
#define NUP 12
#define CODE_BYTES 28672
#define NINI 24
#define STAMP(i)

DI void bar() { asm volatile("s_barrier" ::: "memory"); }
DI void bar_l() { __builtin_amdgcn_s_waitcnt(0xc07f); asm volatile("s_barrier" ::: "memory"); }
template <int N> DI void bar_v() { asm volatile("s_waitcnt vmcnt(%0)\n\ts_barrier" :: "i"(N) : "memory"); }
DI bf16x8 cat(bf16x4 a, bf16x4 b) { return __builtin_shufflevector(a, b, 0, 1, 2, 3, 4, 5, 6, 7); }
DI bf16x8 cat(u32x2 a, u32x2 b) { u32x4 r = {a[0], a[1], b[0], b[1]}; return __builtin_bit_cast(bf16x8, r); }
DI u32x6 cat3(u32x2 a, u32x2 b, u32x2 c) { u32x6 r = {a[0], a[1], b[0], b[1], c[0], c[1]}; return r; }
DI bf16x8 lo4(u32x6 t) { return __builtin_bit_cast(bf16x8, __builtin_shufflevector(t, t, 0, 1, 2, 3)); }
DI bf16x8 hi4(u32x6 t) { return __builtin_bit_cast(bf16x8, __builtin_shufflevector(t, t, 2, 3, 4, 5)); }
DI bf16x4 trd(LAS char* p) { return __builtin_amdgcn_ds_read_tr16_b64_v4bf16((LAS bf16x4*)p); }
DI unsigned pk2(float a, float b) { f32x2 v = {a, b}; return __builtin_bit_cast(unsigned, __builtin_convertvector(v, bf16x2)); }
DI u32x2 pk4(f32x4 v) { u32x2 r = {pk2(v[0], v[1]), pk2(v[2], v[3])}; return r; }
DI f32x4 mfma(bf16x8 a, bf16x8 b, f32x4 c) { return __builtin_amdgcn_mfma_f32_16x16x32_bf16(a, b, c, 0, 0, 0); }
DI float rbf(float v) { return __builtin_bit_cast(float, pk2(v, v) << 16); }
DI u64 mktag(u64 e) { return e * 6364136223846793005ull + 1442695040888963407ull; }
DI unsigned sum4(u32x4 v) { return v[0] + v[1] + v[2] + v[3]; }
DI float fmx(float a, float b) { return __builtin_elementwise_maximum(a, b); }
DI float fbits(unsigned u) { return __builtin_bit_cast(float, u); }
DI unsigned ubits(float f) { return __builtin_bit_cast(unsigned, f); }
template <class F, int... I> DI void sfor_(F&& f, std::integer_sequence<int, I...>) { (f(std::integral_constant<int, I>{}), ...); }
template <int N, class F> DI void sfor(F&& f) { sfor_(f, std::make_integer_sequence<int, N>{}); }
#define IC(v) std::integral_constant<int, (v)>{}

#define PFIT -1
#define NIT 10
#define CARRY_ 4
#define PRE_ 1
#define EGAP_ 5
constexpr int CARRY = CARRY_;
constexpr int PRE = PRE_;
constexpr int EGAP = EGAP_;
constexpr int GATE_F = 4;
constexpr bool has_halo(int W, int f) { return f >= 5 && ((f - 5) & 3) == W; }
constexpr int grp_n(int W, int n) { return (has_halo(W, n % 15) || (W == 2 && n % 15 == GATE_F)) ? 3 : 2; }
constexpr int m_grp(int W, int m) { int n = 30 - CARRY; while (m >= grp_n(W, n % 30)) { m -= grp_n(W, n % 30); ++n; } return n - (30 - CARRY); }
constexpr int m_idx(int W, int m) { int n = 30 - CARRY; while (m >= grp_n(W, n % 30)) { m -= grp_n(W, n % 30); ++n; } return m; }
constexpr int n_mfma(int W) { int t = 0; for (int n = 0; n < 30; ++n) t += grp_n(W, n); return t; }
constexpr int n_read(int W) { return W == 2 ? 68 : 66; }
constexpr int rd_code(int W, int j) {
  constexpr int pos[2][10] = {{0, 3, 10, -1, -1, 29, 32, 41, -1, -1}, {0, 3, 8, -1, 11, 30, 33, 40, -1, 43}};
  int k = 0;
  for (int i = 0; i < 10; ++i) { const int p = pos[W == 2][i]; if (p < 0) continue; if (j == p) return 60 + i; if (j > p) ++k; }
  return j - k;
}

template <int W>
DI void gemm1(LAS char* lds, int lane, int hg, const char* pfb, unsigned pfo, f32x4 (&acc)[2][15], float& logit) {
  const int g = lane >> 4, i16 = lane & 15, q = i16 >> 2, p = lane & 3;
  const unsigned rowb = (16 * (g >> 1) + 4 * (g & 1) + q) * 480;
  const unsigned aM = rowb + 8 * p, aX = rowb + 40 * p + 32;
  const unsigned aA = XOFF + i16 * 128 + ((g ^ (i16 >> 1)) * 16);
  const int rh = i16 < 8 ? 16 + 8 * hg + i16 : i16;
  const unsigned aH = XOFF + rh * 128 + ((g ^ ((rh >> 1) & 7)) * 16);
  f32x4 hal[3];
#pragma unroll
  for (int t = 0; t < 2; ++t)
#pragma unroll
    for (int n = 0; n < 15; ++n) acc[t][n] = f32x4{0, 0, 0, 0};
#pragma unroll
  for (int n = 0; n < 3; ++n) hal[n] = f32x4{0, 0, 0, 0};
  bf16x8 cw[CARRY], cx[3];
  const bf16x8 z8 = __builtin_bit_cast(bf16x8, u32x4{0, 0, 0, 0});
#pragma unroll
  for (int n = 0; n < CARRY; ++n) cw[n] = z8;
#pragma unroll
  for (int n = 0; n < 3; ++n) cx[n] = z8;
  auto one = [&](int f, int i, bf16x8 wf, bf16x8 xf) AI {
    f32x4& d = i < 2 ? acc[i][f] : hal[(f - 5) >> 2];
    d = f < 10 ? mfma(wf, xf, d) : mfma(xf, wf, d);
  };
  constexpr int NM = n_mfma(W), NR = n_read(W), NP = W == 2 ? 7 : 6;
  unsigned buf = 0;
  LAS char* pp[7] = {lds + aM, lds + aX, lds + aA, lds + (aA ^ 64), lds + aH, lds + (aH ^ 64), lds + (GWL + g * 16)};
  int it = NIT, dl = STG;
#pragma unroll 1
  do {
    bf16x4 wh[60]; bf16x8 xf[10];
    auto rd = [&](auto J) AI {
      constexpr int c = rd_code(W, J.value);
      if constexpr (c < 60) {
        constexpr int n = c >> 1, ks = n / 15, f = n % 15, sec = f / 5, jj = f % 5;
        wh[c] = trd((jj < 4 ? pp[0] + 40 * jj : pp[1]) + (ks * 32 + (c & 1) * 8) * 480 + sec * 160);
      } else {
        constexpr int ks = (c - 60) / 5, t = (c - 60) % 5;
        xf[c - 60] = *(LAS bf16x8*)(t < 2 ? pp[2 + ks] + (2 * W + 1 + t) * 2048 : t == 2 ? pp[4 + ks] : pp[6] + ks * 64);
      }
    };
    sfor<NM>([&](auto M) AI {
      constexpr int m = M.value, gi = m_grp(W, m), i = m_idx(W, m);
      if constexpr (m == PRE) { bar_l(); SB(); }
      if constexpr (gi < CARRY) one(15 - CARRY + gi, i, cw[gi], cx[i]);
      else {
        constexpr int n = gi - CARRY, ks = n / 15;
        if constexpr (W == 2 && n % 15 == GATE_F && i == 2) hal[2] = mfma(xf[ks * 5 + 4], xf[ks * 5 + 2], hal[2]);
        else one(n % 15, i, cat(wh[2 * n], wh[2 * n + 1]), xf[ks * 5 + i]);
      }
      SB();
      constexpr int D2 = NR - (NM - EGAP), j = m - PRE;
      if constexpr (j >= 0 && j < D2) { rd(IC(2 * j)); rd(IC(2 * j + 1)); }
      else if constexpr (j >= 0 && D2 + j < NR) rd(IC(D2 + j));
      else if constexpr (j >= 0) {
        constexpr int u = m - (NM - (EGAP - PRE)), NG = EGAP - PRE, PER = (NP + NG - 1) / NG;
        sfor<PER>([&](auto K) AI { constexpr int k = u * PER + K.value; if constexpr (k < NP) { pp[k] += (k == 6 ? 128 : dl); PIN(pp[k]); } });
      }
      if constexpr (W == 3 && PFIT >= 0 && m == NM - 2) {
        if (it == PFIT) asm volatile("s_mov_b32 m0, %2\n\ts_nop 0\n\tglobal_load_lds_dword %0, %1" :: "v"(pfo), "s"(pfb), "s"((unsigned)(size_t)lds + XOFF + 512) : "memory");
      }
      if constexpr (m == 21) { dl = (buf == 2 * STG) ? -2 * STG : STG; asm volatile("" : "+s"(dl)); }
      if constexpr (m == 23) { buf += dl; --it; asm volatile("" : "+s"(buf), "+s"(it)); }
      SB();
    });
#pragma unroll
    for (int n = 0; n < CARRY; ++n) cw[n] = cat(wh[2 * (30 - CARRY + n)], wh[2 * (30 - CARRY + n) + 1]);
#pragma unroll
    for (int n = 0; n < 3; ++n) cx[n] = xf[5 + n];
  } while (it);
#pragma unroll
  for (int gi = 0; gi < CARRY; ++gi)
#pragma unroll
    for (int i = 0; i < 3; ++i)
      if (i < grp_n(W, 30 - CARRY + gi)) one(15 - CARRY + gi, i, cw[gi], cx[i]);
#pragma unroll
  for (int f = 5; f < 15; ++f)
    if (has_halo(W, f)) *(LAS u32x2*)(lds + EXCH + (f - 5) * 512 + lane * 8) = pk4(hal[(f - 5) >> 2]);
  if (W == 2) logit = hal[2][0];
}

struct AttnConst { f32x4 biasC, biasP, biasP0; unsigned selC[2], selP[2], gm[4]; };
DI bf16x8 q_operand(int h, u32x2 qh, u32x2 qx, unsigned gm) { const u32x2 qm = {qx[0] & gm, qx[1] & gm}; return (h & 1) ? cat(qm, qh) : cat(qh, qm); }
DI bf16x8 k_operand(int h, const u32x6 (&k)[2]) { return (h & 1) ? hi4(k[h >> 1]) : lo4(k[h >> 1]); }
#define PACE 3
#define STR2(x) #x
#define STR(x) STR2(x)
#define PW "s_waitcnt vmcnt(" STR(PACE) ")\n\t"
#define DMA(v, srd, so, mo, si, mi) "s_add_u32 m0, " mo ", " #mi "\n\ts_add_u32 %0, " so ", " #si "\n\tbuffer_load_dwordx4 " v ", " srd ", %0 offen lds\n\t"

typedef unsigned u32x16 __attribute__((ext_vector_type(16)));
DI const char* ptr_of(u32x16 k, int i) { return (const char*)(((u64)k[2 * i + 1] << 32) | k[2 * i]); }
DI __amdgpu_buffer_rsrc_t rsrc(const char* p, int bytes) { return __builtin_amdgcn_make_buffer_rsrc((void*)p, 0, bytes, 0x00020000); }

extern "C" __global__ __launch_bounds__(512, 2) void fused_kernel(const char*, const char*, const char*, const char*, const char*, const char*, char*, char*) {
  extern __shared__ __attribute__((aligned(16))) char smem[];
  u32x16 ka;
  asm volatile("s_load_dwordx16 %0, %1, 0x0" : "=&s"(ka) : "s"(__builtin_amdgcn_kernarg_segment_ptr()));
  LAS char* lds = (LAS char*)smem;
  const unsigned ldsb = (unsigned)(size_t)lds;
  const int tid = threadIdx.x, lane = tid & 63;
  const int wid = __builtin_amdgcn_readfirstlane(tid >> 6);
  const int b = blockIdx.x, xcd = b & 7, r = b >> 3;
  const int tg = (xcd & 1) * 8 + (r & 7), hg = (xcd >> 1) * 4 + (r >> 3);
  const int par = tg >> 3, ch = tg & 7;
  const int kq = wid & 3, th = wid >> 2;
#define CNT(i) (void)0
  STAMP(0);
  if (wid == 2) {
    const char* pc; unsigned back; const unsigned len = CODE_BYTES;
    asm volatile("s_getpc_b64 %0\nfk_here:\n\ts_mov_b32 %1, fk_here-fused_kernel" : "=s"(pc), "=s"(back));
    pc -= back;
    const unsigned po = (r * 8 + (lane & 7)) * 128;
    if (lane < 8 && po < len) asm volatile("s_mov_b32 m0, %2\n\ts_nop 0\n\tglobal_load_lds_dword %0, %1" :: "v"(po), "s"(pc), "s"(ldsb + XOFF) : "memory");
  }
  const int stg0 = 2 * ((ch + hg) % 5) + ((par + (hg >> 2)) & 1);
  const int l30 = lane >= 30 ? lane - 30 : lane, sec = (l30 >= 10) + (l30 >= 20);
  unsigned vW = (lane >= 30 ? 3840 : 0) + sec * (1280 - 160) + l30 * 16;
  unsigned vX0 = (lane >> 3) * 2560 + (((lane & 7) ^ (lane >> 4)) * 16), vX1 = vX0 ^ 64;
  unsigned vH = (lane >> 3) * 2560 + (((lane & 7) ^ (4 + kq)) * 16);
  unsigned oW = kq * 16 * 3840 + (hg & 7) * 160;
  unsigned oX = (2 * (128 * ch + 32 * kq) + par) * 1280;
  unsigned oH = (2 * (128 * ch + 2 * kq - (ch ? 8 : 0)) + par) * 1280;
  unsigned sW0 = oW + stg0 * (64 * 3840), sX0 = oX + stg0 * 128, sH0 = oH + stg0 * 128;
  unsigned dW = ldsb + kq * 7680, dX = ldsb + XOFF + (16 + 32 * kq) * 128, dH = ldsb + XOFF + (8 + 2 * kq) * 128;
  asm volatile("s_waitcnt lgkmcnt(0)" : "+s"(ka), "+v"(vW), "+v"(vX0), "+v"(vX1), "+v"(vH), "+s"(sW0), "+s"(sX0), "+s"(sH0), "+s"(dW), "+s"(dX), "+s"(dH));
  const char* const x = ptr_of(ka, 0);
  char* const ws = (char*)ptr_of(ka, 7);

  const auto wsrd = rsrc(hg >= 8 ? ptr_of(ka, 2) : ptr_of(ka, 1), 640 * 3840), xsrd = rsrc(x, 2048 * 1280);
  auto partA = [&](unsigned sw_, unsigned dw) AI {
    unsigned t;
    asm volatile(
      "s_mov_b32 exec_hi, 0x0fffffff\n\t"
      DMA("%1", "%2", "%3", "%4", 0, 0) DMA("%1", "%2", "%3", "%4", 7680, 960) DMA("%1", "%2", "%3", "%4", 30720, 1920)
      DMA("%1", "%2", "%3", "%4", 38400, 2880) DMA("%1", "%2", "%3", "%4", 15360, 3840) DMA("%1", "%2", "%3", "%4", 23040, 4800)
      "s_mov_b32 exec_hi, -1"
      : "=&s"(t) : "v"(vW), "s"(wsrd), "s"(sw_), "s"(dw) : "scc", "memory");
  };
  auto partB = [&](unsigned sw_, unsigned sx, unsigned sh, unsigned dw, unsigned dx, unsigned dh) AI {
    unsigned t;
    asm volatile(
      DMA("%2", "%6", "%8", "%11", 0, 0) DMA("%3", "%6", "%8", "%11", 20480, 1024) DMA("%2", "%6", "%8", "%11", 40960, 2048) DMA("%3", "%6", "%8", "%11", 61440, 3072)
      "s_mov_b32 exec_hi, 0x0fffffff\n\t"
      DMA("%1", "%5", "%7", "%10", 46080, 5760) DMA("%1", "%5", "%7", "%10", 53760, 6720)
      "s_mov_b64 exec, 0xffff\n\t"
      DMA("%4", "%6", "%9", "%12", 0, 0)
      "s_mov_b64 exec, -1"
      : "=&s"(t)
      : "v"(vW), "v"(vX0), "v"(vX1), "v"(vH), "s"(wsrd), "s"(xsrd), "s"(sw_), "s"(sx), "s"(sh), "s"(dw), "s"(dx), "s"(dh)
      : "scc", "memory");
  };
  if (wid >= 4) {
    STAMP(14);
    partB(sW0, sX0, sH0, dW, dX, dH); partA(sW0, dW);
    if (kq < 2) {
      const int c = kq * 64 + lane, sg = stg0 + (c >> 3);
      unsigned t;
      asm volatile(DMA("%1", "%2", "0", "%3", 0, 0) : "=&s"(t)
                   : "v"(c < 80 ? (sg >= 10 ? sg - 10 : sg) * 128 + (c & 7) * 16 : 2048), "s"(rsrc(ptr_of(ka, 3), 1280)), "s"(ldsb + GWL + kq * 1024) : "scc", "memory");
    }
    partA(stg0 == 9 ? oW : sW0 + 64 * 3840, dW + STG);
    STAMP(9);
  }
  SB();
  u64 epoch;
  const int g = lane >> 4, i16 = lane & 15;
  const int hf = (hg >> 2) & 1, ct = (hg & 3) + 4 * (hg >> 3);
  unsigned so[4][3];
  LAS char *pw, *pw3;
  auto setup3 = [&]() AI {
    int tq = tg; asm volatile("" : "+s"(tq));
#pragma unroll
    for (int i = 0; i < 4; ++i) {
      const unsigned ob = ((tq * 16 + 4 * kq + ((tq + i) & 3)) * 4 + 2 * hf + th) * REC;
      so[i][0] = ob + (tq & 4) * 256; so[i][1] = ob + ((tq & 4) ^ 4) * 256; so[i][2] = ob + 4096;
      asm volatile("" : "+s"(so[i][0]), "+s"(so[i][1]), "+s"(so[i][2]));
    }
  };
  unsigned oo[3], go[3]; LAS char* pwr;
  auto setup4 = [&]() AI {
    int td = tid; PIN(td);
#pragma unroll
    for (int e = 0; e < 3; ++e) {
      const int c = e == 1 ? 256 + (td & 63) : td < 320 ? td : 0, tk = c / 10, tok = tk + (e == 2 ? 32 : 0), j = c - tk * 10;
      oo[e] = (2 * (128 * ch + 64 * hf + tok) + par) * 1280 + ct * 160 + j * 16; PIN(oo[e]);
      go[e] = WS_GATE + (tg * 128 + 64 * hf + tok) * 8; PIN(go[e]);
    }
    pwr = lds + ((th ? 0 : 122880) + kq * 10240 + ((td >> 4) & 1) * 5120 + (td & 15) * 160 + ((td >> 5) & 1) * 16);
    PIN(pwr);
  };
  bf16x8 pre[4][5];
  auto pre_read = [&]() AI {
    int ln = lane; PIN(ln);
    pw = lds + ((ln >> 2) * 160 + 8 * (ln & 3) + kq * 5120); pw3 = pw + 3 * 20480;
    PIN(pw); PIN(pw3);
#pragma unroll
    for (int ps = 0; ps < 4; ++ps)
#pragma unroll
      for (int c5 = 0; c5 < 5; ++c5) pre[ps][c5] = cat(trd(pw3 + ps * 20480 + c5 * 32), trd(pw3 + ps * 20480 + c5 * 32 + 2560));
  };
  u32x4 late[3][5];

  u64 gv[3];
  auto project = [&](auto THC) AI {
    constexpr bool TH = THC.value;
    STAMP(4);
    const u64 tag = mktag(epoch);
    const auto orsrc = rsrc(ws + WS_O, O_BYTES);
    struct Rec { u32x4 m[2][2], x; unsigned cs; };
    Rec rec[4];
    const unsigned l16 = lane * 16, l4 = lane * 4;
    auto ldrec = [&](int i, int j) AI {
      if (j < 4) rec[i].m[j >> 1][j & 1] = __builtin_amdgcn_raw_buffer_load_b128(orsrc, l16 + (j & 1) * 2048, so[i][j >> 1], 16);
      else if (j == 4) rec[i].x = __builtin_amdgcn_raw_buffer_load_b128(orsrc, l16, so[i][2], 16);
      else rec[i].cs = __builtin_amdgcn_raw_buffer_load_b32(orsrc, l4 + 1024, so[i][2], 16);
    };
    auto reload = [&](int i) AI {
      Rec& q = rec[i];
      asm volatile("buffer_load_dwordx4 %0, %6, %8, %9 offen sc1\n\tbuffer_load_dwordx4 %1, %6, %8, %9 offen offset:2048 sc1\n\t"
                   "buffer_load_dwordx4 %2, %6, %8, %10 offen sc1\n\tbuffer_load_dwordx4 %3, %6, %8, %10 offen offset:2048 sc1\n\t"
                   "buffer_load_dwordx4 %4, %6, %8, %11 offen sc1\n\tbuffer_load_dword %5, %7, %8, %11 offen offset:1024 sc1\n\ts_waitcnt vmcnt(0)"
                   : "+v"(q.m[0][0]), "+v"(q.m[0][1]), "+v"(q.m[1][0]), "+v"(q.m[1][1]), "+v"(q.x), "+v"(q.cs)
                   : "v"(l16), "v"(l4), "s"(orsrc), "s"(so[i][0]), "s"(so[i][1]), "s"(so[i][2]) : "memory");
    };
    auto word = [&](int i, int k) AI { return k < 16 ? rec[i].m[k >> 3][(k >> 2) & 1][k & 3] : rec[i].x[k & 3]; };
    auto stale = [&](int i) AI {
      unsigned c = (unsigned)tag;
#pragma unroll
      for (int k = 0; k < 20; ++k) c += word(i, k);
      return __builtin_amdgcn_ballot_w64(c != rec[i].cs);
    };
    auto refetch = [&](int i, int n) AI {
#pragma unroll 1
      for (int spin = 0; spin < SPIN; ++spin) { CNT(i); reload(i); if (!stale(i)) break; }
#pragma unroll
      for (int k = 1; k < n; ++k) reload(i + k);
    };
    __builtin_amdgcn_s_setprio(TH ? 1 : 2);
    if constexpr (TH) {
      LAS char* lp = lds + 3 * 20480 + kq * 5120 + lane * 16;
#pragma unroll
      for (int ps = 0; ps < 3; ++ps)
#pragma unroll
        for (int i = 0; i < 5; ++i) *(LAS u32x4*)(lp + ((ps + 2) % 3) * 20480 + i * 1024) = late[(ps + 2) % 3][i];
      SB(); setup3(); setup4();
      bar_l();
    }
    SB();
    constexpr int NU = TH ? NUP : NUC, ST = TH ? 1 : CSTEP;
    sfor<NU>([&](auto J) AI { ldrec(J.value / 6, J.value % 6); SB(); });
    STAMP(6);
    SB();
    bf16x4 wq[10][10];
    auto rdq = [&](auto P, auto I) AI {
      constexpr int G[10] = {3, 4, 5, 6, 0, 1, 2, 3, 4, 5};
      wq[P.value][I.value] = trd((G[P.value] < 3 ? pw + G[P.value] * 20480 : pw3 + (G[P.value] - 3) * 20480) + (I.value >> 1) * 32 + (I.value & 1) * 2560);
    };
    SB();
    if (__builtin_expect(stale(0) != 0, 0)) refetch(0, NU / 6);
    if constexpr (TH && LV) { if (__builtin_expect(stale(1) != 0, 0)) refetch(1, NU / 6 - 1); }
    STAMP(5);
    f32x4 a3[2][5];
    unsigned csum = 0; u64 bad = 0; bf16x8 xo[2];
    sfor<100>([&](auto M) AI {
      constexpr int m = M.value;
      constexpr int pos = m < 70 ? m / 10 : 7 + ((m - 70) % 6) / 2, c5 = m < 70 ? (m % 10) >> 1 : (m - 70) / 6, t = m & 1;
      constexpr int pp = pos < 4 ? pos : pos - 1;
      if constexpr ((m == 20 && !(TH && LV)) || m == 50 || m == 70) { constexpr int i = m == 20 ? 1 : m == 50 ? 2 : 3; if (__builtin_expect(bad != 0, 0)) refetch(i, i == 1 ? 1 : 4 - i); SB(); }
      if constexpr (m == 80 && TH) { bar(); SB(); }
      const bf16x8 ov = (pos == 4 || pos == 9) ? xo[t] : __builtin_bit_cast(bf16x8, rec[pp >> 1].m[pp & 1][t]);
      const bf16x8 wv = pos < 4 ? pre[pos][c5] : cat(wq[pos][2 * c5], wq[pos][2 * c5 + 1]);
      if constexpr (m < 10) asm volatile("v_mfma_f32_16x16x32_bf16 %0, %1, %2, 0" : "=&v"(a3[t][c5]) : "v"(wv), "v"(ov));
      else asm volatile("v_mfma_f32_16x16x32_bf16 %0, %1, %2, %0" : "+v"(a3[t][c5]) : "v"(wv), "v"(ov));
      SB();
      if constexpr (m % ST == 0 && m / ST < 24 - NU) ldrec((NU + m / ST) / 6, (NU + m / ST) % 6);
      if constexpr (!TH && m == BPOS) bar();
      if constexpr (!TH && m > (24 - NU) * ST && m <= (24 - NU) * ST + 3) gv[m - (24 - NU) * ST - 1] = __hip_atomic_load((const GAS u64*)(ws + go[m - (24 - NU) * ST - 1]), RLX);
      if constexpr (TH && m == 24 - NU) { gv[2] = __hip_atomic_load((const GAS u64*)(ws + go[2]), RLX); gv[0] = gv[1] = gv[2]; }
      if constexpr (m >= 20 && m < 50) rdq(IC(m / 10 + 2), IC(m % 10));
      if constexpr (m >= 50 && m < 80) rdq(IC(7 + ((m - 50) % 6) / 2), IC(2 * ((m - 50) / 6) + (m & 1)));
      constexpr int ci = m < 20 ? 1 : m < 50 ? 2 : 3, cb = m < 20 ? 8 : m < 50 ? 36 : 56, k = m - cb;
      if constexpr (m < 70 && (m >= 20 || !(TH && LV)) && k >= 0 && k < 10) { csum = (k ? csum : (unsigned)tag) + word(ci, 2 * k) + word(ci, 2 * k + 1); PIN(csum); }
      if constexpr (m < 70 && (m >= 20 || !(TH && LV)) && k == 10) { bad = __builtin_amdgcn_ballot_w64(csum != rec[ci].cs); asm volatile("" : "+s"(bad)); }
      if constexpr (m == 30 || m == 31 || m == 70 || m == 71) {
        constexpr int r0 = m < 70 ? 0 : 2;
        xo[t] = cat(u32x2{rec[r0].x[2 * t], rec[r0].x[2 * t + 1]}, u32x2{rec[r0 + 1].x[2 * t], rec[r0 + 1].x[2 * t + 1]}); PIN(xo[t]);
      }
      if constexpr (m >= 82 && (m - 82) % 6 < 2) *(LAS f32x4*)(pwr + t * 16 * 160 + ((m - 82) / 6) * 32) = a3[t][(m - 82) / 6];
      SB();
    });
    *(LAS f32x4*)(pwr + 3 * 32) = a3[0][3];
    asm volatile("s_nop 1" : "+v"(a3[1][3]));
    *(LAS f32x4*)(pwr + 16 * 160 + 3 * 32) = a3[1][3];
    asm volatile("s_nop 5" : "+v"(a3[0][4]), "+v"(a3[1][4]));
    *(LAS f32x4*)(pwr + 4 * 32) = a3[0][4];
    *(LAS f32x4*)(pwr + 16 * 160 + 4 * 32) = a3[1][4];
    STAMP(7);
  };

  if (__builtin_expect(wid >= 4, 0)) {
    int s1 = stg0 == 9 ? 0 : stg0 + 1, s2 = stg0 >= 8 ? stg0 - 8 : stg0 + 2;
    asm volatile("s_load_dwordx2 %0, %1, 0x0\n\ts_waitcnt lgkmcnt(0)" : "=&s"(epoch) : "s"(ws + WS_E + tg * 128) : "memory");
    const auto wosrd = rsrc(kq >> 1 ? ptr_of(ka, 5) : ptr_of(ka, 4), 640 * 1280);
    const int rot = tg & 3, sw = (tg >> 2) & 1;
    unsigned vm[5], ve[5];
#pragma unroll
    for (int i = 0; i < 5; ++i) {
      const int c = i * 64 + lane, rr = c / 10, wi = c - rr * 10;
      vm[i] = (20 * (rr >> 4) + (rr & 15)) * 1280 + wi * 16; PIN(vm[i]);
      ve[i] = (20 * ((rr >> 2) & 3) + 16 + (rr & 3)) * 1280 + wi * 16; PIN(ve[i]);
    }
    auto wo_off = [&](int pos, unsigned (&v)[5], unsigned& sq) AI {
      if (pos == 4 || pos == 9) {
        const unsigned ha = (4 * (kq & 1) + ((rot + (pos == 9 ? 2 : 0)) & 3)) * 102400, hb = (4 * (kq & 1) + ((rot + (pos == 9 ? 3 : 1)) & 3)) * 102400;
#pragma unroll
        for (int i = 0; i < 5; ++i) v[i] = ve[i] + ((i * 64 + lane >= 160) ? hb : ha);
        sq = ct * 160;
      } else {
        const int pp = pos < 4 ? pos : pos - 1;
#pragma unroll
        for (int i = 0; i < 5; ++i) v[i] = vm[i];
        sq = ct * 160 + (4 * (kq & 1) + ((rot + (pp >> 1)) & 3)) * 102400 + ((pp & 1) ^ sw) * 51200;
      }
    };
    auto wo_pos = [&](int pos, int grp, bool paced) AI {
      unsigned v[5], sq, t;
      wo_off(pos, v, sq);
#define WO5(P) asm volatile(P DMA("%1", "%6", "%7", "%8", 0, 0) P DMA("%2", "%6", "%7", "%8", 0, 1024) P DMA("%3", "%6", "%7", "%8", 0, 2048) P DMA("%4", "%6", "%7", "%8", 0, 3072) P DMA("%5", "%6", "%7", "%8", 0, 4096) \
                   : "=&s"(t) : "v"(v[0]), "v"(v[1]), "v"(v[2]), "v"(v[3]), "v"(v[4]), "s"(wosrd), "s"(sq), "s"(ldsb + grp * 20480 + kq * 5120) : "scc", "memory")
      if (paced) WO5(PW); else WO5("");
    };
    auto late_load = [&](int ps, bool paced) AI {
      unsigned v[5], sq;
      wo_off(7 + ps, v, sq);
#pragma unroll
      for (int i = 0; i < 5; ++i) { if (paced) asm volatile(PW ::: "memory"); late[ps][i] = __builtin_amdgcn_raw_buffer_load_b128(wosrd, v[i], sq, 0); }
    };
    unsigned b1 = STG, b2 = 2 * STG;
    unsigned q0, q1, q2, q3, q4, q5, q6, q7;
    auto prep = [&]() AI {
      q0 = oW + s1 * (64 * 3840); q1 = oX + s1 * 128; q2 = oH + s1 * 128; q3 = dW + b1; q4 = dX + b1; q5 = dH + b1; q6 = oW + s2 * (64 * 3840); q7 = dW + b2;
      asm volatile("" : "+s"(q0), "+s"(q1), "+s"(q2), "+s"(q3), "+s"(q4), "+s"(q5), "+s"(q6), "+s"(q7));
      s1 = s2; s2 = (s2 == 9) ? 0 : s2 + 1;
      b1 = b2; b2 = (b2 == 2 * STG) ? 0 : b2 + STG;
    };
    prep();
    asm volatile("s_waitcnt vmcnt(6)" ::: "memory");
#pragma unroll 1
    for (int it = 0; it < 8; ++it) {
      SB(); bar(); SB();
      partB(q0, q1, q2, q3, q4, q5); partA(q6, q7);
      SB();
      prep();
      asm volatile("s_waitcnt vmcnt(6)" ::: "memory");
    }
    bar();
    STAMP(19);
    partB(q0, q1, q2, q3, q4, q5);
    wo_pos(0, 3, false);
    bar_v<5>();
    STAMP(13);
    wo_pos(1, 4, false); wo_pos(2, 5, false); wo_pos(3, 6, false);
    late_load(2, false);
    STAMP(1);
    bar();
    STAMP(2);
    wo_pos(4, 0, UNP < 1); wo_pos(5, 1, UNP < 2); wo_pos(6, 2, UNP < 3);
    late_load(0, true);
    STAMP(3);
    bar();
    late_load(1, false);
    pre_read();
    __builtin_amdgcn_s_waitcnt(0xc07f);
    bar_v<10>();
    project(IC(1));
  } else {
    asm volatile("s_load_dwordx2 %0, %1, 0x0" : "=&s"(epoch) : "s"(ws + WS_E + tg * 128) : "memory");
    const float NINF = -__builtin_inff();
    AttnConst ac;
    const bool first = (ch == 0) && (wid == 0);
#pragma unroll
    for (int j = 0; j < 4; ++j) {
      const int c = 4 * g + j;
      ac.biasC[j] = (c <= i16 && c >= i16 - 7) ? 0.f : NINF;
      ac.biasP[j] = (c >= i16 + 9) ? 0.f : NINF;
      ac.biasP0[j] = first ? NINF : ac.biasP[j];
      ac.gm[j] = (g == j) ? 0xffffffffu : 0u; PIN(ac.gm[j]);
    }
#pragma unroll
    for (int j = 0; j < 2; ++j) {
      const bool c0 = 4 * g + 2 * j <= i16, c1 = 4 * g + 2 * j + 1 <= i16;
      ac.selC[j] = (c0 ? 0x0302u : 0x0c0cu) | (c1 ? 0x07060000u : 0x0c0c0000u); PIN(ac.selC[j]);
      ac.selP[j] = (c0 ? 0x0c0cu : 0x0302u) | (c1 ? 0x0c0c0000u : 0x07060000u); PIN(ac.selP[j]);
    }
    PIN(ac.biasC); PIN(ac.biasP); PIN(ac.biasP0);
    setup3();
    if (wid == 1) {
      const unsigned o1 = (2 * (128 * ch + 64 * hf) + par) * 1280 + (lane & 1) * 126 * 1280;
      if (lane < 2) asm volatile("s_mov_b32 m0, %2\n\ts_nop 0\n\tglobal_load_lds_dword %0, %1" :: "v"(o1), "s"(ptr_of(ka, 6)), "s"(ldsb + XOFF + 256) : "memory");
    }
    const char* pfb = b >> 7 ? ptr_of(ka, 5) : ptr_of(ka, 4);
    unsigned pfo = (b & 127) * 6400 + (lane < 50 ? lane : 49) * 128; PIN(pfo);
    f32x4 acc[2][15]; float logit = 0.f;
    STAMP(9);
    if (wid == 0) gemm1<0>(lds, lane, hg, pfb, pfo, acc, logit);
    else if (wid == 1) gemm1<1>(lds, lane, hg, pfb, pfo, acc, logit);
    else if (wid == 2) gemm1<2>(lds, lane, hg, pfb, pfo, acc, logit);
    else gemm1<3>(lds, lane, hg, pfb, pfo, acc, logit);
    asm volatile("s_waitcnt vmcnt(0)" : "+s"(epoch) :: "memory");
    STAMP(1);
    const auto orsrc = rsrc(ws + WS_O, O_BYTES);
    u32x6 K[3][2], V[5];
    auto set2 = [&](u32x6& t, int i, u32x2 v) AI { t[i] = v[0]; t[i + 1] = v[1]; };
    K[2][0] = cat3(pk4(acc[1][5]), pk4(acc[1][9]), pk4(acc[1][6])); PIN(K[2][0]);
    K[2][1] = cat3(pk4(acc[1][7]), u32x2{K[2][0][2], K[2][0][3]}, pk4(acc[1][8])); PIN(K[2][1]);
#pragma unroll
    for (int f = 0; f < 5; ++f) { asm volatile("" : "=v"(V[f])); set2(V[f], 4, pk4(acc[1][10 + f])); PIN(V[f]); }
    if (wid < 3) {
      LAS char* e = lds + EXCH + (wid + 1) * 5120 + lane * 8;
      *(LAS u32x2*)(e) = u32x2{K[2][0][0], K[2][0][1]}; *(LAS u32x2*)(e + 512) = u32x2{K[2][0][4], K[2][0][5]};
      *(LAS u32x2*)(e + 1024) = u32x2{K[2][1][0], K[2][1][1]}; *(LAS u32x2*)(e + 1536) = u32x2{K[2][1][4], K[2][1][5]};
      *(LAS u32x2*)(e + 2048) = u32x2{K[2][0][2], K[2][0][3]};
#pragma unroll
      for (int f = 0; f < 5; ++f) *(LAS u32x2*)(e + (5 + f) * 512) = u32x2{V[f][4], V[f][5]};
    }
    SB();
    const u32x2 qx[2] = {pk4(acc[0][4]), pk4(acc[1][4])};
    bf16x8 qb[2][4]; f32x4 sc[2][4], sp[2][4];
    auto scores = [&](int p) AI {
      K[1][p] = cat3(pk4(acc[0][5 + 2 * p]), p ? u32x2{K[1][0][2], K[1][0][3]} : pk4(acc[0][9]), pk4(acc[0][6 + 2 * p])); PIN(K[1][p]);
#pragma unroll
      for (int t = 1; t >= 0; --t)
#pragma unroll
        for (int h = 2 * p; h < 2 * p + 2; ++h) {
          qb[t][h] = q_operand(h, pk4(acc[t][h]), qx[t], ac.gm[h]);
          sc[t][h] = mfma(k_operand(h, K[t + 1]), qb[t][h], ac.biasC);
          if (t) sp[t][h] = mfma(k_operand(h, K[t]), qb[t][h], ac.biasP);
        }
    };
    scores(0);
    SB();
    bar_l();
    STAMP(2);
    SB();
    {
      LAS char* e = lds + EXCH + wid * 5120 + lane * 8;
      K[0][0] = cat3(*(volatile LAS u32x2*)(e), *(volatile LAS u32x2*)(e + 2048), *(volatile LAS u32x2*)(e + 512));
      set2(V[0], 0, *(volatile LAS u32x2*)(e + 5 * 512)); set2(V[1], 0, *(volatile LAS u32x2*)(e + 6 * 512)); set2(V[4], 0, *(volatile LAS u32x2*)(e + 9 * 512));
      K[0][1] = cat3(*(volatile LAS u32x2*)(e + 1024), *(volatile LAS u32x2*)(e + 2048), *(volatile LAS u32x2*)(e + 1536));
      set2(V[2], 0, *(volatile LAS u32x2*)(e + 7 * 512)); set2(V[3], 0, *(volatile LAS u32x2*)(e + 8 * 512));
    }
    const unsigned obase = ((tg * 16 + hg) * 4 + wid) * REC;
    unsigned cs = (unsigned)mktag(epoch);
    auto put = [&](int k, u32x4 v) AI {
      __builtin_amdgcn_raw_buffer_store_b128(v, orsrc, lane * 16 + (k & 3) * 1024, obase + (k >> 2) * 4096, OS);
      cs += sum4(v);
    };
    const float CS = 0.22360679774997896f * 1.4426950408889634f;
    const bf16x8 ones = __builtin_bit_cast(bf16x8, u32x4{0x3f803f80u, 0x3f803f80u, 0x3f803f80u, 0x3f803f80u});
    const f32x4 z4 = {0, 0, 0, 0};
    f32x4 s[2][4], l[2][4], o[2][4], e[2][4]; float mc[2][4], rl[2][4]; u32x2 op[2][4];
    auto maxima = [&](int p) AI {
      unsigned ml[4];
#pragma unroll
      for (int h = 2 * p; h < 2 * p + 2; ++h) { if (h == 2 * p) PIN(K[0][p]); sp[0][h] = mfma(k_operand(h, K[0]), qb[0][h], ac.biasP0); }
#pragma unroll
      for (int u = 3; u >= 0; --u) {
        const int t = u >> 1, h = 2 * p + (u & 1);
        s[t][h] = __builtin_elementwise_maximum(sc[t][h], sp[t][h]);
        ml[u] = ubits(fmx(fmx(fmx(s[t][h][0], s[t][h][1]), s[t][h][2]), s[t][h][3]));
      }
      auto r2 = __builtin_amdgcn_permlane16_swap(ml[2], ml[3], false, false);
      auto r1 = __builtin_amdgcn_permlane16_swap(ml[0], ml[1], false, false);
      auto r3 = __builtin_amdgcn_permlane32_swap(ubits(fmx(fbits(r1[0]), fbits(r1[1]))), ubits(fmx(fbits(r2[0]), fbits(r2[1]))), false, false);
      const unsigned w = ubits(fmx(fbits(r3[0]), fbits(r3[1])) * -CS);
      auto r4 = __builtin_amdgcn_permlane32_swap(w, w, false, false);
      auto r5 = __builtin_amdgcn_permlane16_swap(r4[0], r4[0], false, false);
      auto r6 = __builtin_amdgcn_permlane16_swap(r4[1], r4[1], false, false);
      mc[0][2 * p] = fbits(r5[0]); mc[0][2 * p + 1] = fbits(r5[1]); mc[1][2 * p] = fbits(r6[0]); mc[1][2 * p + 1] = fbits(r6[1]);
    };
    auto weigh = [&](int h) AI {
#pragma unroll
      for (int t = 1; t >= 0; --t) {
        const f32x4 a = __builtin_elementwise_fma(s[t][h], f32x4{CS, CS, CS, CS}, f32x4{mc[t][h], mc[t][h], mc[t][h], mc[t][h]});
        unsigned pr[4];
#pragma unroll
        for (int j = 0; j < 4; ++j) pr[j] = ubits(__builtin_amdgcn_exp2f(a[j]));
        const bf16x8 pb = cat(u32x2{__builtin_amdgcn_perm(pr[1], pr[0], ac.selP[0]), __builtin_amdgcn_perm(pr[3], pr[2], ac.selP[1])},
                              u32x2{__builtin_amdgcn_perm(pr[1], pr[0], ac.selC[0]), __builtin_amdgcn_perm(pr[3], pr[2], ac.selC[1])});
        l[t][h] = mfma(ones, pb, z4);
        o[t][h] = mfma(t ? hi4(V[h]) : lo4(V[h]), pb, z4);
        e[t][h] = mfma(t ? hi4(V[4]) : lo4(V[4]), pb, z4);
      }
    };
    auto emit = [&](int p) AI {
#pragma unroll
      for (int t = 1; t >= 0; --t) {
#pragma unroll
        for (int h = 2 * p; h < 2 * p + 2; ++h) { rl[t][h] = __builtin_amdgcn_rcpf(l[t][h][0]); op[t][h] = pk4(o[t][h] * rl[t][h]); }
        put(2 * t + p, u32x4{op[t][2 * p][0], op[t][2 * p][1], op[t][2 * p + 1][0], op[t][2 * p + 1][1]});
      }
    };
    set2(V[0], 2, pk4(acc[0][10])); set2(V[1], 2, pk4(acc[0][11])); set2(V[4], 2, pk4(acc[0][14]));
    maxima(0);
    PIN(V[0]); PIN(V[1]); PIN(V[4]);
    weigh(0); weigh(1); SB();
    scores(1); emit(0); SB();
    set2(V[2], 2, pk4(acc[0][12])); set2(V[3], 2, pk4(acc[0][13]));
    maxima(1);
    PIN(V[2]); PIN(V[3]);
    u32x4 ex[2]; unsigned rx[2];
#pragma unroll
    for (int t = 1; t >= 0; --t) {
      ex[t] = (__builtin_bit_cast(u32x4, e[t][1]) & ac.gm[1]) | (__builtin_bit_cast(u32x4, e[t][0]) & ~ac.gm[1]);
      rx[t] = (ubits(rl[t][1]) & ac.gm[1]) | (ubits(rl[t][0]) & ~ac.gm[1]);
    }
    weigh(2); weigh(3); SB();
    if (wid == 2) {
      const float gt = rbf(__builtin_amdgcn_rcpf(1.f + __expf(-rbf(logit))));
      if (lane < 8)
        __hip_atomic_store((GAS u64*)(ws + WS_GATE + (tg * 128 + 8 * hg + lane) * 8), ((u64)(unsigned)mktag(epoch) << 32) | ubits(gt), __ATOMIC_RELAXED, OS == 17 ? __HIP_MEMORY_SCOPE_SYSTEM : __HIP_MEMORY_SCOPE_AGENT);
    }
    emit(1); SB();
    u32x2 ox[2];
#pragma unroll
    for (int t = 1; t >= 0; --t) {
#pragma unroll
      for (int h = 2; h < 4; ++h) { ex[t] = (__builtin_bit_cast(u32x4, e[t][h]) & ac.gm[h]) | (ex[t] & ~ac.gm[h]); rx[t] = (ubits(rl[t][h]) & ac.gm[h]) | (rx[t] & ~ac.gm[h]); }
      ox[t] = pk4(__builtin_bit_cast(f32x4, ex[t]) * fbits(rx[t]));
    }
    put(4, u32x4{ox[0][0], ox[0][1], ox[1][0], ox[1][1]});
    {
      const u32x4 c4 = {(unsigned)__builtin_amdgcn_mov_dpp((int)cs, 0x00, 0xf, 0xf, true), (unsigned)__builtin_amdgcn_mov_dpp((int)cs, 0x55, 0xf, 0xf, true),
                        (unsigned)__builtin_amdgcn_mov_dpp((int)cs, 0xaa, 0xf, 0xf, true), (unsigned)__builtin_amdgcn_mov_dpp((int)cs, 0xff, 0xf, 0xf, true)};
      if ((lane & 3) == 0) __builtin_amdgcn_raw_buffer_store_b128(c4, orsrc, lane * 4, obase + 5120, OS);
    }
    STAMP(3);
    setup4();
    bar();
    pre_read();
    STAMP(10);
    asm volatile("s_waitcnt vmcnt(0)" ::: "memory");
    STAMP(11);
    bar_l();
    project(IC(0));
  }
  const u64 tag = mktag(epoch);
  if (__builtin_expect(__builtin_amdgcn_ballot_w64(((unsigned)(gv[0] >> 32) ^ (unsigned)tag) | ((unsigned)(gv[1] >> 32) ^ (unsigned)tag) | ((unsigned)(gv[2] >> 32) ^ (unsigned)tag)) != 0, 0)) {
#pragma unroll
    for (int e = 0; e < 3; ++e)
#pragma unroll 1
      for (int spin = 0; spin < SPIN; ++spin) { gv[e] = __hip_atomic_load((const GAS u64*)(ws + go[e]), RLX); if (__builtin_amdgcn_ballot_w64((unsigned)(gv[e] >> 32) != (unsigned)tag) == 0) break; }
  }
  char* const out = (char*)ptr_of(ka, 6);
  auto finish = [&](auto E, LAS char* pr) AI {
    constexpr int e = E.value;
    const float gt = fbits((unsigned)gv[e]), gn = rbf(1.f - gt);
    const f32x4 lo = (*(LAS f32x4*)(pr) + *(LAS f32x4*)(pr + 10240)) * gt + (*(LAS f32x4*)(pr + 20480) + *(LAS f32x4*)(pr + 30720)) * gn;
    const f32x4 hi = (*(LAS f32x4*)(pr + 5120) + *(LAS f32x4*)(pr + 15360)) * gt + (*(LAS f32x4*)(pr + 25600) + *(LAS f32x4*)(pr + 35840)) * gn;
    const u32x2 l2 = pk4(lo), h2 = pk4(hi);
    if ((e < 2 ? OA0 : OA1) < 0) *(GAS u32x4*)(out + oo[e]) = u32x4{l2[0], l2[1], h2[0], h2[1]};
    else __builtin_amdgcn_raw_buffer_store_b128(u32x4{l2[0], l2[1], h2[0], h2[1]}, rsrc(out, 2048 * 1280), oo[e], 0, e < 2 ? (OA0 < 0 ? 0 : OA0) : (OA1 < 0 ? 0 : OA1));
  };
  if (!th) {
    __builtin_amdgcn_s_setprio(0);
    bar_l();
    finish(IC(0), lds + 122880 + tid * 16);
    if (wid == 0) finish(IC(1), lds + 122880 + (256 + lane) * 16);
  }
  bar_l();
  if (wid < 5) finish(IC(2), lds + tid * 16);
  if (hg == 0 && tid == 0) { if (ES) __hip_atomic_store((GAS u64*)(ws + WS_E + tg * 128), tag, __ATOMIC_RELAXED, __HIP_MEMORY_SCOPE_SYSTEM); else *(GAS u64*)(ws + WS_E + tg * 128) = tag; }
  STAMP(8);
}

extern "C" void kernel_launch(void* const* d_in, const int* in_sizes, int n_in, void* d_out, int out_size, void* d_ws, size_t ws_size, hipStream_t stream) {
  (void)in_sizes; (void)n_in; (void)out_size; (void)ws_size;
  (void)hipFuncSetAttribute((const void*)fused_kernel, hipFuncAttributeMaxDynamicSharedMemorySize, LDS_BYTES);
  fused_kernel<<<256, 512, LDS_BYTES, stream>>>((const char*)d_in[0], (const char*)d_in[1], (const char*)d_in[4], (const char*)d_in[6], (const char*)d_in[2],
                                                (const char*)d_in[5], (char*)d_out, (char*)d_ws);
}
```

```cpp
#include <hip/hip_runtime.h>
#include <hip/hip_bf16.h>
#include <utility>

typedef __bf16 bf16x8 __attribute__((ext_vector_type(8)));
typedef __bf16 bf16x4 __attribute__((ext_vector_type(4)));
typedef __bf16 bf16x2 __attribute__((ext_vector_type(2)));
typedef float f32x4 __attribute__((ext_vector_type(4)));
typedef float f32x2 __attribute__((ext_vector_type(2)));
typedef unsigned u32x6 __attribute__((ext_vector_type(6)));
typedef unsigned u32x4 __attribute__((ext_vector_type(4)));
typedef unsigned u32x2 __attribute__((ext_vector_type(2)));
typedef unsigned long long u64;
#define LAS __attribute__((address_space(3)))
#define GAS __attribute__((address_space(1)))
#define DI __device__ __forceinline__
#define AI __attribute__((always_inline))
#define SB() __builtin_amdgcn_sched_barrier(0)
#define PIN(x) asm volatile("" : "+v"(x))
#define RLX __ATOMIC_RELAXED, __HIP_MEMORY_SCOPE_AGENT

constexpr int STG = 49152;
constexpr int XOFF = 30720;
constexpr int EXCH = 143360;
constexpr int GWL = EXCH + 3 * 5120;
constexpr int LDS_BYTES = 163840;
constexpr int WS_E = 0;
constexpr int WS_GATE = 4096;
constexpr int WS_O = 32768;
constexpr int REC = 5376;
constexpr int O_BYTES = 16 * 16 * 4 * REC;
constexpr int SPIN = 1 << 16;
constexpr int SYS = 17, DEV = 16;
constexpr unsigned CODE_BYTES = 28672;

DI void bar() { asm volatile("s_barrier" ::: "memory"); }
DI void bar_l() { __builtin_amdgcn_s_waitcnt(0xc07f); asm volatile("s_barrier" ::: "memory"); }
template <int N> DI void bar_v() { asm volatile("s_waitcnt vmcnt(%0)\n\ts_barrier" :: "i"(N) : "memory"); }
DI bf16x8 cat(bf16x4 a, bf16x4 b) { return __builtin_shufflevector(a, b, 0, 1, 2, 3, 4, 5, 6, 7); }
DI bf16x8 cat(u32x2 a, u32x2 b) { u32x4 r = {a[0], a[1], b[0], b[1]}; return __builtin_bit_cast(bf16x8, r); }
DI u32x6 cat3(u32x2 a, u32x2 b, u32x2 c) { u32x6 r = {a[0], a[1], b[0], b[1], c[0], c[1]}; return r; }
DI bf16x8 lo4(u32x6 t) { return __builtin_bit_cast(bf16x8, __builtin_shufflevector(t, t, 0, 1, 2, 3)); }
DI bf16x8 hi4(u32x6 t) { return __builtin_bit_cast(bf16x8, __builtin_shufflevector(t, t, 2, 3, 4, 5)); }
DI bf16x4 trd(LAS char* p) { return __builtin_amdgcn_ds_read_tr16_b64_v4bf16((LAS bf16x4*)p); }
DI unsigned pk2(float a, float b) { f32x2 v = {a, b}; return __builtin_bit_cast(unsigned, __builtin_convertvector(v, bf16x2)); }
DI u32x2 pk4(f32x4 v) { u32x2 r = {pk2(v[0], v[1]), pk2(v[2], v[3])}; return r; }
DI f32x4 mfma(bf16x8 a, bf16x8 b, f32x4 c) { return __builtin_amdgcn_mfma_f32_16x16x32_bf16(a, b, c, 0, 0, 0); }
DI float rbf(float v) { return __builtin_bit_cast(float, pk2(v, v) << 16); }
DI u64 mktag(u64 e) { return e * 6364136223846793005ull + 1442695040888963407ull; }
DI unsigned sum4(u32x4 v) { return v[0] + v[1] + v[2] + v[3]; }
DI float fmx(float a, float b) { return __builtin_elementwise_maximum(a, b); }
DI float fbits(unsigned u) { return __builtin_bit_cast(float, u); }
DI unsigned ubits(float f) { return __builtin_bit_cast(unsigned, f); }
template <class F, int... I> DI void sfor_(F&& f, std::integer_sequence<int, I...>) { (f(std::integral_constant<int, I>{}), ...); }
template <int N, class F> DI void sfor(F&& f) { sfor_(f, std::make_integer_sequence<int, N>{}); }
#define IC(v) std::integral_constant<int, (v)>{}

constexpr int CARRY = 4;
constexpr int PRE = 1;
constexpr int EGAP = 5;
constexpr int GATE_F = 4;
constexpr bool has_halo(int W, int f) { return f >= 5 && ((f - 5) & 3) == W; }
constexpr int grp_n(int W, int n) { return (has_halo(W, n % 15) || (W == 2 && n % 15 == GATE_F)) ? 3 : 2; }
constexpr int m_grp(int W, int m) { int n = 30 - CARRY; while (m >= grp_n(W, n % 30)) { m -= grp_n(W, n % 30); ++n; } return n - (30 - CARRY); }
constexpr int m_idx(int W, int m) { int n = 30 - CARRY; while (m >= grp_n(W, n % 30)) { m -= grp_n(W, n % 30); ++n; } return m; }
constexpr int n_mfma(int W) { int t = 0; for (int n = 0; n < 30; ++n) t += grp_n(W, n); return t; }
constexpr int n_read(int W) { return W == 2 ? 68 : 66; }
constexpr int rd_code(int W, int j) {
  constexpr int pos[2][10] = {{0, 3, 10, -1, -1, 29, 32, 41, -1, -1}, {0, 3, 8, -1, 11, 30, 33, 40, -1, 43}};
  int k = 0;
  for (int i = 0; i < 10; ++i) { const int p = pos[W == 2][i]; if (p < 0) continue; if (j == p) return 60 + i; if (j > p) ++k; }
  return j - k;
}

template <int W>
DI void gemm1(LAS char* lds, int lane, int hg, f32x4 (&acc)[2][15], float& logit) {
  const int g = lane >> 4, i16 = lane & 15, q = i16 >> 2, p = lane & 3;
  const unsigned rowb = (16 * (g >> 1) + 4 * (g & 1) + q) * 480;
  const unsigned aM = rowb + 8 * p, aX = rowb + 40 * p + 32;
  const unsigned aA = XOFF + i16 * 128 + ((g ^ (i16 >> 1)) * 16);
  const int rh = i16 < 8 ? 16 + 8 * hg + i16 : i16;
  const unsigned aH = XOFF + rh * 128 + ((g ^ ((rh >> 1) & 7)) * 16);
  f32x4 hal[3];
#pragma unroll
  for (int t = 0; t < 2; ++t)
#pragma unroll
    for (int n = 0; n < 15; ++n) acc[t][n] = f32x4{0, 0, 0, 0};
#pragma unroll
  for (int n = 0; n < 3; ++n) hal[n] = f32x4{0, 0, 0, 0};
  bf16x8 cw[CARRY], cx[3];
  const bf16x8 z8 = __builtin_bit_cast(bf16x8, u32x4{0, 0, 0, 0});
#pragma unroll
  for (int n = 0; n < CARRY; ++n) cw[n] = z8;
#pragma unroll
  for (int n = 0; n < 3; ++n) cx[n] = z8;
  auto one = [&](int f, int i, bf16x8 wf, bf16x8 xf) AI {
    f32x4& d = i < 2 ? acc[i][f] : hal[(f - 5) >> 2];
    d = f < 10 ? mfma(wf, xf, d) : mfma(xf, wf, d);
  };
  constexpr int NM = n_mfma(W), NR = n_read(W), NP = W == 2 ? 7 : 6;
  unsigned buf = 0;
  LAS char* pp[7] = {lds + aM, lds + aX, lds + aA, lds + (aA ^ 64), lds + aH, lds + (aH ^ 64), lds + (GWL + g * 16)};
  int it = 10, dl = STG;
#pragma unroll 1
  do {
    bf16x4 wh[60]; bf16x8 xf[10];
    auto rd = [&](auto J) AI {
      constexpr int c = rd_code(W, J.value);
      if constexpr (c < 60) {
        constexpr int n = c >> 1, ks = n / 15, f = n % 15, sec = f / 5, jj = f % 5;
        wh[c] = trd((jj < 4 ? pp[0] + 40 * jj : pp[1]) + (ks * 32 + (c & 1) * 8) * 480 + sec * 160);
      } else {
        constexpr int ks = (c - 60) / 5, t = (c - 60) % 5;
        xf[c - 60] = *(LAS bf16x8*)(t < 2 ? pp[2 + ks] + (2 * W + 1 + t) * 2048 : t == 2 ? pp[4 + ks] : pp[6] + ks * 64);
      }
    };
    sfor<NM>([&](auto M) AI {
      constexpr int m = M.value, gi = m_grp(W, m), i0 = m_idx(W, m), i = (i0 < 2 && (gi & 1)) ? 1 - i0 : i0;
      if constexpr (m == PRE) { bar_l(); SB(); }
      if constexpr (gi < CARRY) one(15 - CARRY + gi, i, cw[gi], cx[i]);
      else {
        constexpr int n = gi - CARRY, ks = n / 15;
        if constexpr (W == 2 && n % 15 == GATE_F && i == 2) hal[2] = mfma(xf[ks * 5 + 4], xf[ks * 5 + 2], hal[2]);
        else one(n % 15, i, cat(wh[2 * n], wh[2 * n + 1]), xf[ks * 5 + i]);
      }
      SB();
      constexpr int D2 = NR - (NM - EGAP), j = m - PRE;
      if constexpr (j >= 0 && j < D2) { rd(IC(2 * j)); rd(IC(2 * j + 1)); }
      else if constexpr (j >= 0 && D2 + j < NR) rd(IC(D2 + j));
      else if constexpr (j >= 0) {
        constexpr int u = m - (NM - (EGAP - PRE)), NG = EGAP - PRE, PER = (NP + NG - 1) / NG;
        sfor<PER>([&](auto K) AI { constexpr int k = u * PER + K.value; if constexpr (k < NP) { pp[k] += (k == 6 ? 128 : dl); PIN(pp[k]); } });
      }
      if constexpr (m == 21) { dl = (buf == 2 * STG) ? -2 * STG : STG; asm volatile("" : "+s"(dl)); }
      if constexpr (m == 23) { buf += dl; --it; asm volatile("" : "+s"(buf), "+s"(it)); }
      SB();
    });
#pragma unroll
    for (int n = 0; n < CARRY; ++n) cw[n] = cat(wh[2 * (30 - CARRY + n)], wh[2 * (30 - CARRY + n) + 1]);
#pragma unroll
    for (int n = 0; n < 3; ++n) cx[n] = xf[5 + n];
  } while (it);
#pragma unroll
  for (int gi = 0; gi < CARRY; ++gi)
#pragma unroll
    for (int i = 0; i < 3; ++i)
      if (i < grp_n(W, 30 - CARRY + gi)) one(15 - CARRY + gi, i, cw[gi], cx[i]);
#pragma unroll
  for (int f = 5; f < 15; ++f)
    if (has_halo(W, f)) *(LAS u32x2*)(lds + EXCH + (f - 5) * 512 + lane * 8) = pk4(hal[(f - 5) >> 2]);
  if (W == 2) logit = hal[2][0];
}

struct AttnConst { f32x4 biasC, biasP, biasP0; unsigned selC[2], selP[2], gm[4]; };
DI bf16x8 q_operand(int h, u32x2 qh, u32x2 qx, unsigned gm) { const u32x2 qm = {qx[0] & gm, qx[1] & gm}; return (h & 1) ? cat(qm, qh) : cat(qh, qm); }
DI bf16x8 k_operand(int h, const u32x6 (&k)[2]) { return (h & 1) ? hi4(k[h >> 1]) : lo4(k[h >> 1]); }
#define PW "s_waitcnt vmcnt(3)\n\t"
#define DMA(v, srd, so, mo, si, mi) "s_add_u32 m0, " mo ", " #mi "\n\ts_add_u32 %0, " so ", " #si "\n\tbuffer_load_dwordx4 " v ", " srd ", %0 offen lds\n\t"

typedef unsigned u32x16 __attribute__((ext_vector_type(16)));
DI const char* ptr_of(u32x16 k, int i) { return (const char*)(((u64)k[2 * i + 1] << 32) | k[2 * i]); }
DI __amdgpu_buffer_rsrc_t rsrc(const char* p, int bytes) { return __builtin_amdgcn_make_buffer_rsrc((void*)p, 0, bytes, 0x00020000); }

extern "C" __global__ __launch_bounds__(512, 2) void fused_kernel(const char*, const char*, const char*, const char*, const char*, const char*, char*, char*) {
  extern __shared__ __attribute__((aligned(16))) char smem[];
  u32x16 ka;
  asm volatile("s_load_dwordx16 %0, %1, 0x0" : "=&s"(ka) : "s"(__builtin_amdgcn_kernarg_segment_ptr()));
  LAS char* lds = (LAS char*)smem;
  const unsigned ldsb = (unsigned)(size_t)lds;
  const int tid = threadIdx.x, lane = tid & 63;
  const int wid = __builtin_amdgcn_readfirstlane(tid >> 6);
  const int b = blockIdx.x, xcd = b & 7, r = b >> 3;
  const int tg = (xcd & 1) * 8 + (r & 7), hg = (xcd >> 1) * 4 + (r >> 3);
  const int par = tg >> 3, ch = tg & 7;
  const int kq = wid & 3, th = wid >> 2;
  if (wid == 2) {
    const char* pc; unsigned back;
    asm volatile("s_getpc_b64 %0\nfk_here:\n\ts_mov_b32 %1, fk_here-fused_kernel" : "=s"(pc), "=s"(back));
    pc -= back;
    const unsigned po = (r * 8 + (lane & 7)) * 128;
    if (lane < 8 && po < CODE_BYTES) asm volatile("s_mov_b32 m0, %2\n\ts_nop 0\n\tglobal_load_lds_dword %0, %1" :: "v"(po), "s"(pc), "s"(ldsb + XOFF) : "memory");
  }
  const int stg0 = 2 * ((ch + hg) % 5) + ((par + (hg >> 2)) & 1);
  const int l30 = lane >= 30 ? lane - 30 : lane, sec = (l30 >= 10) + (l30 >= 20);
  unsigned vW = (lane >= 30 ? 3840 : 0) + sec * (1280 - 160) + l30 * 16;
  unsigned vX0 = (lane >> 3) * 2560 + (((lane & 7) ^ (lane >> 4)) * 16), vX1 = vX0 ^ 64;
  unsigned vH = (lane >> 3) * 2560 + (((lane & 7) ^ (4 + kq)) * 16);
  unsigned oW = kq * 16 * 3840 + (hg & 7) * 160;
  unsigned oX = (2 * (128 * ch + 32 * kq) + par) * 1280;
  unsigned oH = (2 * (128 * ch + 2 * kq - (ch ? 8 : 0)) + par) * 1280;
  unsigned sW0 = oW + stg0 * (64 * 3840), sX0 = oX + stg0 * 128, sH0 = oH + stg0 * 128;
  unsigned dW = ldsb + kq * 7680, dX = ldsb + XOFF + (16 + 32 * kq) * 128, dH = ldsb + XOFF + (8 + 2 * kq) * 128;
  asm volatile("s_waitcnt lgkmcnt(0)" : "+s"(ka), "+v"(vW), "+v"(vX0), "+v"(vX1), "+v"(vH), "+s"(sW0), "+s"(sX0), "+s"(sH0), "+s"(dW), "+s"(dX), "+s"(dH));
  const char* const x = ptr_of(ka, 0);
  char* const ws = (char*)ptr_of(ka, 7);
  u64 epoch, epoch_in;
  asm volatile("s_load_dwordx2 %0, %1, 0x0" : "=&s"(epoch_in) : "s"(ws + WS_E + tg * 128) : "memory");

  const auto wsrd = rsrc(hg >= 8 ? ptr_of(ka, 2) : ptr_of(ka, 1), 640 * 3840), xsrd = rsrc(x, 2048 * 1280);
  auto partA = [&](unsigned sw_, unsigned dw) AI {
    unsigned t;
    asm volatile(
      "s_mov_b32 exec_hi, 0x0fffffff\n\t"
      DMA("%1", "%2", "%3", "%4", 0, 0) DMA("%1", "%2", "%3", "%4", 7680, 960) DMA("%1", "%2", "%3", "%4", 30720, 1920)
      DMA("%1", "%2", "%3", "%4", 38400, 2880) DMA("%1", "%2", "%3", "%4", 15360, 3840) DMA("%1", "%2", "%3", "%4", 23040, 4800)
      "s_mov_b32 exec_hi, -1"
      : "=&s"(t) : "v"(vW), "s"(wsrd), "s"(sw_), "s"(dw) : "scc", "memory");
  };
  auto partB = [&](unsigned sw_, unsigned sx, unsigned sh, unsigned dw, unsigned dx, unsigned dh) AI {
    unsigned t;
    asm volatile(
      DMA("%2", "%6", "%8", "%11", 0, 0) DMA("%3", "%6", "%8", "%11", 20480, 1024) DMA("%2", "%6", "%8", "%11", 40960, 2048) DMA("%3", "%6", "%8", "%11", 61440, 3072)
      "s_mov_b32 exec_hi, 0x0fffffff\n\t"
      DMA("%1", "%5", "%7", "%10", 46080, 5760) DMA("%1", "%5", "%7", "%10", 53760, 6720)
      "s_mov_b64 exec, 0xffff\n\t"
      DMA("%4", "%6", "%9", "%12", 0, 0)
      "s_mov_b64 exec, -1"
      : "=&s"(t)
      : "v"(vW), "v"(vX0), "v"(vX1), "v"(vH), "s"(wsrd), "s"(xsrd), "s"(sw_), "s"(sx), "s"(sh), "s"(dw), "s"(dx), "s"(dh)
      : "scc", "memory");
  };
  if (wid >= 4) {
    partB(sW0, sX0, sH0, dW, dX, dH); partA(sW0, dW);
    if (kq < 2) {
      const int c = kq * 64 + lane, sg = stg0 + (c >> 3);
      unsigned t;
      asm volatile(DMA("%1", "%2", "0", "%3", 0, 0) : "=&s"(t)
                   : "v"(c < 80 ? (sg >= 10 ? sg - 10 : sg) * 128 + (c & 7) * 16 : 2048), "s"(rsrc(ptr_of(ka, 3), 1280)), "s"(ldsb + GWL + kq * 1024) : "scc", "memory");
    }
    partA(stg0 == 9 ? oW : sW0 + 64 * 3840, dW + STG);
  }
  SB();
  const int g = lane >> 4, i16 = lane & 15;
  const int hf = (hg >> 2) & 1, ct = (hg & 3) + 4 * (hg >> 3);
  unsigned so[4][3];
  LAS char *pw, *pw3;
  auto setup3 = [&]() AI {
    int tq = tg; asm volatile("" : "+s"(tq));
#pragma unroll
    for (int i = 0; i < 4; ++i) {
      const unsigned ob = ((tq * 16 + 4 * kq + ((tq + i) & 3)) * 4 + 2 * hf + th) * REC;
      so[i][0] = ob + (tq & 4) * 256; so[i][1] = ob + ((tq & 4) ^ 4) * 256; so[i][2] = ob + 4096;
      asm volatile("" : "+s"(so[i][0]), "+s"(so[i][1]), "+s"(so[i][2]));
    }
  };
  unsigned oo[3], go[3]; LAS char *pwr, *prd[3];
  auto setup4 = [&]() AI {
    int td = tid; PIN(td);
#pragma unroll
    for (int e = 0; e < 3; ++e) {
      const int c = e == 1 ? 256 + (td & 63) : td < 320 ? td : 0, tk = c / 10, tok = tk + (e == 2 ? 32 : 0), j = c - tk * 10;
      oo[e] = (2 * (128 * ch + 64 * hf + tok) + par) * 1280 + ct * 160 + j * 16; PIN(oo[e]);
      go[e] = WS_GATE + (tg * 128 + 64 * hf + tok) * 8; PIN(go[e]);
      prd[e] = lds + ((e == 2 ? 0 : 122880) + (c ^ ((tk >> 2) & 1)) * 16); PIN(prd[e]);
    }
    pwr = lds + ((th ? 0 : 122880) + kq * 10240 + ((td >> 4) & 1) * 5120 + (td & 15) * 160 + (((td >> 5) ^ (td >> 2)) & 1) * 16);
    PIN(pwr);
  };
  bf16x8 pre[4][5];
  auto pre_read = [&]() AI {
    int ln = lane; PIN(ln);
    pw = lds + ((ln >> 2) * 160 + 8 * (ln & 3) + kq * 5120); pw3 = pw + 3 * 20480;
    PIN(pw); PIN(pw3);
#pragma unroll
    for (int ps = 0; ps < 4; ++ps)
#pragma unroll
      for (int c5 = 0; c5 < 5; ++c5) pre[ps][c5] = cat(trd(pw3 + ps * 20480 + c5 * 32), trd(pw3 + ps * 20480 + c5 * 32 + 2560));
  };
  u32x4 late[3][5];

  u64 gv[3];
  auto project = [&](auto THC) AI {
    constexpr bool TH = THC.value;
    const u64 tag = mktag(epoch);
    const auto orsrc = rsrc(ws + WS_O, O_BYTES);
    struct Rec { u32x4 m[2][2], x; unsigned cs; };
    Rec rec[4];
    const unsigned l16 = lane * 16, l4 = lane * 4;
    auto ldrec = [&](int i, int j) AI {
      if (j < 4) rec[i].m[j >> 1][j & 1] = __builtin_amdgcn_raw_buffer_load_b128(orsrc, l16 + (j & 1) * 2048, so[i][j >> 1], DEV);
      else if (j == 4) rec[i].x = __builtin_amdgcn_raw_buffer_load_b128(orsrc, l16, so[i][2], DEV);
      else rec[i].cs = __builtin_amdgcn_raw_buffer_load_b32(orsrc, l4 + 1024, so[i][2], DEV);
    };
    auto reload = [&](int i) AI {
      Rec& q = rec[i];
      asm volatile("buffer_load_dwordx4 %0, %6, %8, %9 offen sc1\n\tbuffer_load_dwordx4 %1, %6, %8, %9 offen offset:2048 sc1\n\t"
                   "buffer_load_dwordx4 %2, %6, %8, %10 offen sc1\n\tbuffer_load_dwordx4 %3, %6, %8, %10 offen offset:2048 sc1\n\t"
                   "buffer_load_dwordx4 %4, %6, %8, %11 offen sc1\n\tbuffer_load_dword %5, %7, %8, %11 offen offset:1024 sc1\n\ts_waitcnt vmcnt(0)"
                   : "+v"(q.m[0][0]), "+v"(q.m[0][1]), "+v"(q.m[1][0]), "+v"(q.m[1][1]), "+v"(q.x), "+v"(q.cs)
                   : "v"(l16), "v"(l4), "s"(orsrc), "s"(so[i][0]), "s"(so[i][1]), "s"(so[i][2]) : "memory");
    };
    auto word = [&](int i, int k) AI { return k < 16 ? rec[i].m[k >> 3][(k >> 2) & 1][k & 3] : rec[i].x[k & 3]; };
    auto stale = [&](int i) AI {
      unsigned c = (unsigned)tag;
#pragma unroll
      for (int k = 0; k < 20; ++k) c += word(i, k);
      return __builtin_amdgcn_ballot_w64(c != rec[i].cs);
    };
    auto refetch = [&](int i, int n) AI {
#pragma unroll 1
      for (int spin = 0; spin < SPIN; ++spin) { reload(i); if (!stale(i)) break; }
#pragma unroll
      for (int k = 1; k < n; ++k) reload(i + k);
    };
    __builtin_amdgcn_s_setprio(TH ? 1 : 2);
    if constexpr (TH) {
      LAS char* lp = lds + 3 * 20480 + kq * 5120 + lane * 16;
#pragma unroll
      for (int ps = 0; ps < 3; ++ps)
#pragma unroll
        for (int i = 0; i < 5; ++i) *(LAS u32x4*)(lp + ((ps + 2) % 3) * 20480 + i * 1024) = late[(ps + 2) % 3][i];
      SB(); setup3(); setup4();
      bar_l();
    }
    SB();
    sfor<12>([&](auto J) AI { ldrec(J.value / 6, J.value % 6); SB(); });
    SB();
    bf16x4 wq[10][10];
    auto rdq = [&](auto P, auto I) AI {
      constexpr int G[10] = {3, 4, 5, 6, 0, 1, 2, 3, 4, 5};
      wq[P.value][I.value] = trd((G[P.value] < 3 ? pw + G[P.value] * 20480 : pw3 + (G[P.value] - 3) * 20480) + (I.value >> 1) * 32 + (I.value & 1) * 2560);
    };
    SB();
    if (__builtin_expect(stale(0) != 0, 0)) refetch(0, 2);
    if constexpr (TH) { if (__builtin_expect(stale(1) != 0, 0)) refetch(1, 1); }
    f32x4 a3[2][5];
    unsigned csum = 0; u64 bad = 0; bf16x8 xo[2];
    sfor<100>([&](auto M) AI {
      constexpr int m = M.value;
      constexpr int pos = m < 70 ? m / 10 : 7 + ((m - 70) % 6) / 2, c5 = m < 70 ? (m % 10) >> 1 : (m - 70) / 6, t = m & 1;
      constexpr int pp = pos < 4 ? pos : pos - 1;
      if constexpr ((m == 20 && !TH) || m == 50 || m == 70) { constexpr int i = m == 20 ? 1 : m == 50 ? 2 : 3; if (__builtin_expect(bad != 0, 0)) refetch(i, i == 1 ? 1 : 4 - i); SB(); }
      if constexpr (m == 80 && TH) { bar(); SB(); }
      const bf16x8 ov = (pos == 4 || pos == 9) ? xo[t] : __builtin_bit_cast(bf16x8, rec[pp >> 1].m[pp & 1][t]);
      const bf16x8 wv = pos < 4 ? pre[pos][c5] : cat(wq[pos][2 * c5], wq[pos][2 * c5 + 1]);
      if constexpr (m < 10) asm volatile("v_mfma_f32_16x16x32_bf16 %0, %1, %2, 0" : "=&v"(a3[t][c5]) : "v"(wv), "v"(ov));
      else asm volatile("v_mfma_f32_16x16x32_bf16 %0, %1, %2, %0" : "+v"(a3[t][c5]) : "v"(wv), "v"(ov));
      SB();
      if constexpr (m < 12) ldrec(2 + m / 6, m % 6);
      if constexpr (!TH && m == 12) bar();
      if constexpr (!TH && m > 12 && m < 16) gv[m - 13] = __hip_atomic_load((const GAS u64*)(ws + go[m - 13]), RLX);
      if constexpr (TH && m == 12) { gv[2] = __hip_atomic_load((const GAS u64*)(ws + go[2]), RLX); gv[0] = gv[1] = gv[2]; }
      if constexpr (m >= 20 && m < 50) rdq(IC(m / 10 + 2), IC(m % 10));
      if constexpr (m >= 50 && m < 80) rdq(IC(7 + ((m - 50) % 6) / 2), IC(2 * ((m - 50) / 6) + (m & 1)));
      constexpr int ci = m < 20 ? 1 : m < 50 ? 2 : 3, cb = m < 20 ? 8 : m < 50 ? 36 : 56, k = m - cb;
      if constexpr (m < 70 && (m >= 20 || !TH) && k >= 0 && k < 10) { csum = (k ? csum : (unsigned)tag) + word(ci, 2 * k) + word(ci, 2 * k + 1); PIN(csum); }
      if constexpr (m < 70 && (m >= 20 || !TH) && k == 10) { bad = __builtin_amdgcn_ballot_w64(csum != rec[ci].cs); asm volatile("" : "+s"(bad)); }
      if constexpr (m == 30 || m == 31 || m == 70 || m == 71) {
        constexpr int r0 = m < 70 ? 0 : 2;
        xo[t] = cat(u32x2{rec[r0].x[2 * t], rec[r0].x[2 * t + 1]}, u32x2{rec[r0 + 1].x[2 * t], rec[r0 + 1].x[2 * t + 1]}); PIN(xo[t]);
      }
      if constexpr (m >= 82 && (m - 82) % 6 < 2) *(LAS f32x4*)(pwr + t * 16 * 160 + ((m - 82) / 6) * 32) = a3[t][(m - 82) / 6];
      SB();
    });
    *(LAS f32x4*)(pwr + 3 * 32) = a3[0][3];
    asm volatile("s_nop 1" : "+v"(a3[1][3]));
    *(LAS f32x4*)(pwr + 16 * 160 + 3 * 32) = a3[1][3];
    asm volatile("s_nop 5" : "+v"(a3[0][4]), "+v"(a3[1][4]));
    *(LAS f32x4*)(pwr + 4 * 32) = a3[0][4];
    *(LAS f32x4*)(pwr + 16 * 160 + 4 * 32) = a3[1][4];
  };

  if (__builtin_expect(wid >= 4, 0)) {
    int s1 = stg0 == 9 ? 0 : stg0 + 1, s2 = stg0 >= 8 ? stg0 - 8 : stg0 + 2;
    const auto wosrd = rsrc(kq >> 1 ? ptr_of(ka, 5) : ptr_of(ka, 4), 640 * 1280);
    const int rot = tg & 3, sw = (tg >> 2) & 1;
    unsigned vm[5], ve[5];
#pragma unroll
    for (int i = 0; i < 5; ++i) {
      const int c = i * 64 + lane, rr = c / 10, wi = c - rr * 10;
      vm[i] = (20 * (rr >> 4) + (rr & 15)) * 1280 + wi * 16; PIN(vm[i]);
      ve[i] = (20 * ((rr >> 2) & 3) + 16 + (rr & 3)) * 1280 + wi * 16; PIN(ve[i]);
    }
    auto wo_off = [&](int pos, unsigned (&v)[5], unsigned& sq) AI {
      if (pos == 4 || pos == 9) {
        const unsigned ha = (4 * (kq & 1) + ((rot + (pos == 9 ? 2 : 0)) & 3)) * 102400, hb = (4 * (kq & 1) + ((rot + (pos == 9 ? 3 : 1)) & 3)) * 102400;
#pragma unroll
        for (int i = 0; i < 5; ++i) v[i] = ve[i] + ((i * 64 + lane >= 160) ? hb : ha);
        sq = ct * 160;
      } else {
        const int pp = pos < 4 ? pos : pos - 1;
#pragma unroll
        for (int i = 0; i < 5; ++i) v[i] = vm[i];
        sq = ct * 160 + (4 * (kq & 1) + ((rot + (pp >> 1)) & 3)) * 102400 + ((pp & 1) ^ sw) * 51200;
      }
    };
    auto wo_pos = [&](int pos, int grp, bool paced) AI {
      unsigned v[5], sq, t;
      wo_off(pos, v, sq);
#define WO5(P) asm volatile(P DMA("%1", "%6", "%7", "%8", 0, 0) P DMA("%2", "%6", "%7", "%8", 0, 1024) P DMA("%3", "%6", "%7", "%8", 0, 2048) P DMA("%4", "%6", "%7", "%8", 0, 3072) P DMA("%5", "%6", "%7", "%8", 0, 4096) \
                   : "=&s"(t) : "v"(v[0]), "v"(v[1]), "v"(v[2]), "v"(v[3]), "v"(v[4]), "s"(wosrd), "s"(sq), "s"(ldsb + grp * 20480 + kq * 5120) : "scc", "memory")
      if (paced) WO5(PW); else WO5("");
    };
    auto late_load = [&](int ps, bool paced) AI {
      unsigned v[5], sq;
      wo_off(7 + ps, v, sq);
#pragma unroll
      for (int i = 0; i < 5; ++i) { if (paced) asm volatile(PW ::: "memory"); late[ps][i] = __builtin_amdgcn_raw_buffer_load_b128(wosrd, v[i], sq, 0); }
    };
    unsigned b1 = STG, b2 = 2 * STG;
    unsigned q0, q1, q2, q3, q4, q5, q6, q7;
    auto prep = [&]() AI {
      q0 = oW + s1 * (64 * 3840); q1 = oX + s1 * 128; q2 = oH + s1 * 128; q3 = dW + b1; q4 = dX + b1; q5 = dH + b1; q6 = oW + s2 * (64 * 3840); q7 = dW + b2;
      asm volatile("" : "+s"(q0), "+s"(q1), "+s"(q2), "+s"(q3), "+s"(q4), "+s"(q5), "+s"(q6), "+s"(q7));
      s1 = s2; s2 = (s2 == 9) ? 0 : s2 + 1;
      b1 = b2; b2 = (b2 == 2 * STG) ? 0 : b2 + STG;
    };
    prep();
    asm volatile("s_waitcnt vmcnt(6) lgkmcnt(0)\n\ts_mov_b64 %0, %1" : "=s"(epoch) : "s"(epoch_in) : "memory");
#pragma unroll 1
    for (int it = 0; it < 8; ++it) {
      SB(); bar(); SB();
      partB(q0, q1, q2, q3, q4, q5); partA(q6, q7);
      SB();
      prep();
      asm volatile("s_waitcnt vmcnt(6)" ::: "memory");
    }
    bar();
    partB(q0, q1, q2, q3, q4, q5);
    wo_pos(0, 3, false);
    bar_v<5>();
    wo_pos(1, 4, false); wo_pos(2, 5, false); wo_pos(3, 6, false);
    late_load(2, false);
    bar();
    wo_pos(4, 0, false); wo_pos(5, 1, true); wo_pos(6, 2, true);
    late_load(0, true);
    bar();
    late_load(1, false);
    pre_read();
    __builtin_amdgcn_s_waitcnt(0xc07f);
    bar_v<10>();
    project(IC(1));
  } else {
    const float NINF = -__builtin_inff();
    AttnConst ac;
    const bool first = (ch == 0) && (wid == 0);
#pragma unroll
    for (int j = 0; j < 4; ++j) {
      const int c = 4 * g + j;
      ac.biasC[j] = (c <= i16 && c >= i16 - 7) ? 0.f : NINF;
      ac.biasP[j] = (c >= i16 + 9) ? 0.f : NINF;
      ac.biasP0[j] = first ? NINF : ac.biasP[j];
      ac.gm[j] = (g == j) ? 0xffffffffu : 0u; PIN(ac.gm[j]);
    }
#pragma unroll
    for (int j = 0; j < 2; ++j) {
      const bool c0 = 4 * g + 2 * j <= i16, c1 = 4 * g + 2 * j + 1 <= i16;
      ac.selC[j] = (c0 ? 0x0302u : 0x0c0cu) | (c1 ? 0x07060000u : 0x0c0c0000u); PIN(ac.selC[j]);
      ac.selP[j] = (c0 ? 0x0c0cu : 0x0302u) | (c1 ? 0x0c0c0000u : 0x07060000u); PIN(ac.selP[j]);
    }
    PIN(ac.biasC); PIN(ac.biasP); PIN(ac.biasP0);
    setup3();
    f32x4 acc[2][15]; float logit = 0.f;
    asm volatile("s_waitcnt lgkmcnt(0)\n\ts_mov_b64 %0, %1" : "=s"(epoch) : "s"(epoch_in) : "memory");
    if (wid == 0) gemm1<0>(lds, lane, hg, acc, logit);
    else if (wid == 1) gemm1<1>(lds, lane, hg, acc, logit);
    else if (wid == 2) gemm1<2>(lds, lane, hg, acc, logit);
    else gemm1<3>(lds, lane, hg, acc, logit);
    asm volatile("s_waitcnt vmcnt(0)" ::: "memory");
    const auto orsrc = rsrc(ws + WS_O, O_BYTES);
    u32x6 K[3][2], V[5];
    auto set2 = [&](u32x6& t, int i, u32x2 v) AI { t[i] = v[0]; t[i + 1] = v[1]; };
    K[2][0] = cat3(pk4(acc[1][5]), pk4(acc[1][9]), pk4(acc[1][6])); PIN(K[2][0]);
    K[2][1] = cat3(pk4(acc[1][7]), u32x2{K[2][0][2], K[2][0][3]}, pk4(acc[1][8])); PIN(K[2][1]);
#pragma unroll
    for (int f = 0; f < 5; ++f) { asm volatile("" : "=v"(V[f])); set2(V[f], 4, pk4(acc[1][10 + f])); PIN(V[f]); }
    if (wid < 3) {
      LAS char* e = lds + EXCH + (wid + 1) * 5120 + lane * 8;
      *(LAS u32x2*)(e) = u32x2{K[2][0][0], K[2][0][1]}; *(LAS u32x2*)(e + 512) = u32x2{K[2][0][4], K[2][0][5]};
      *(LAS u32x2*)(e + 1024) = u32x2{K[2][1][0], K[2][1][1]}; *(LAS u32x2*)(e + 1536) = u32x2{K[2][1][4], K[2][1][5]};
      *(LAS u32x2*)(e + 2048) = u32x2{K[2][0][2], K[2][0][3]};
#pragma unroll
      for (int f = 0; f < 5; ++f) *(LAS u32x2*)(e + (5 + f) * 512) = u32x2{V[f][4], V[f][5]};
    }
    SB();
    const u32x2 qx[2] = {pk4(acc[0][4]), pk4(acc[1][4])};
    bf16x8 qb[2][4]; f32x4 sc[2][4], sp[2][4];
    auto scores = [&](int p) AI {
      K[1][p] = cat3(pk4(acc[0][5 + 2 * p]), p ? u32x2{K[1][0][2], K[1][0][3]} : pk4(acc[0][9]), pk4(acc[0][6 + 2 * p])); PIN(K[1][p]);
#pragma unroll
      for (int t = 1; t >= 0; --t)
#pragma unroll
        for (int h = 2 * p; h < 2 * p + 2; ++h) {
          qb[t][h] = q_operand(h, pk4(acc[t][h]), qx[t], ac.gm[h]);
          sc[t][h] = mfma(k_operand(h, K[t + 1]), qb[t][h], ac.biasC);
          if (t) sp[t][h] = mfma(k_operand(h, K[t]), qb[t][h], ac.biasP);
        }
    };
    scores(0);
    SB();
    bar_l();
    SB();
    {
      LAS char* e = lds + EXCH + wid * 5120 + lane * 8;
      K[0][0] = cat3(*(volatile LAS u32x2*)(e), *(volatile LAS u32x2*)(e + 2048), *(volatile LAS u32x2*)(e + 512));
      set2(V[0], 0, *(volatile LAS u32x2*)(e + 5 * 512)); set2(V[1], 0, *(volatile LAS u32x2*)(e + 6 * 512)); set2(V[4], 0, *(volatile LAS u32x2*)(e + 9 * 512));
      K[0][1] = cat3(*(volatile LAS u32x2*)(e + 1024), *(volatile LAS u32x2*)(e + 2048), *(volatile LAS u32x2*)(e + 1536));
      set2(V[2], 0, *(volatile LAS u32x2*)(e + 7 * 512)); set2(V[3], 0, *(volatile LAS u32x2*)(e + 8 * 512));
    }
    const unsigned obase = ((tg * 16 + hg) * 4 + wid) * REC;
    unsigned cs = (unsigned)mktag(epoch);
    auto put = [&](int k, u32x4 v) AI {
      __builtin_amdgcn_raw_buffer_store_b128(v, orsrc, lane * 16 + (k & 3) * 1024, obase + (k >> 2) * 4096, SYS);
      cs += sum4(v);
    };
    const float CS = 0.22360679774997896f * 1.4426950408889634f;
    const bf16x8 ones = __builtin_bit_cast(bf16x8, u32x4{0x3f803f80u, 0x3f803f80u, 0x3f803f80u, 0x3f803f80u});
    const f32x4 z4 = {0, 0, 0, 0};
    f32x4 s[2][4], l[2][4], o[2][4], e[2][4]; float mc[2][4], rl[2][4]; u32x2 op[2][4];
    auto maxima = [&](int p) AI {
      unsigned ml[4];
#pragma unroll
      for (int h = 2 * p; h < 2 * p + 2; ++h) { if (h == 2 * p) PIN(K[0][p]); sp[0][h] = mfma(k_operand(h, K[0]), qb[0][h], ac.biasP0); }
#pragma unroll
      for (int u = 3; u >= 0; --u) {
        const int t = u >> 1, h = 2 * p + (u & 1);
        s[t][h] = __builtin_elementwise_maximum(sc[t][h], sp[t][h]);
        ml[u] = ubits(fmx(fmx(fmx(s[t][h][0], s[t][h][1]), s[t][h][2]), s[t][h][3]));
      }
      auto r2 = __builtin_amdgcn_permlane16_swap(ml[2], ml[3], false, false);
      auto r1 = __builtin_amdgcn_permlane16_swap(ml[0], ml[1], false, false);
      auto r3 = __builtin_amdgcn_permlane32_swap(ubits(fmx(fbits(r1[0]), fbits(r1[1]))), ubits(fmx(fbits(r2[0]), fbits(r2[1]))), false, false);
      const unsigned w = ubits(fmx(fbits(r3[0]), fbits(r3[1])) * -CS);
      auto r4 = __builtin_amdgcn_permlane32_swap(w, w, false, false);
      auto r5 = __builtin_amdgcn_permlane16_swap(r4[0], r4[0], false, false);
      auto r6 = __builtin_amdgcn_permlane16_swap(r4[1], r4[1], false, false);
      mc[0][2 * p] = fbits(r5[0]); mc[0][2 * p + 1] = fbits(r5[1]); mc[1][2 * p] = fbits(r6[0]); mc[1][2 * p + 1] = fbits(r6[1]);
    };
    auto weigh = [&](int h) AI {
#pragma unroll
      for (int t = 1; t >= 0; --t) {
        const f32x4 a = __builtin_elementwise_fma(s[t][h], f32x4{CS, CS, CS, CS}, f32x4{mc[t][h], mc[t][h], mc[t][h], mc[t][h]});
        unsigned pr[4];
#pragma unroll
        for (int j = 0; j < 4; ++j) pr[j] = ubits(__builtin_amdgcn_exp2f(a[j]));
        const bf16x8 pb = cat(u32x2{__builtin_amdgcn_perm(pr[1], pr[0], ac.selP[0]), __builtin_amdgcn_perm(pr[3], pr[2], ac.selP[1])},
                              u32x2{__builtin_amdgcn_perm(pr[1], pr[0], ac.selC[0]), __builtin_amdgcn_perm(pr[3], pr[2], ac.selC[1])});
        l[t][h] = mfma(ones, pb, z4);
        o[t][h] = mfma(t ? hi4(V[h]) : lo4(V[h]), pb, z4);
        e[t][h] = mfma(t ? hi4(V[4]) : lo4(V[4]), pb, z4);
      }
    };
    auto emit = [&](int p) AI {
#pragma unroll
      for (int t = 1; t >= 0; --t) {
#pragma unroll
        for (int h = 2 * p; h < 2 * p + 2; ++h) { rl[t][h] = __builtin_amdgcn_rcpf(l[t][h][0]); op[t][h] = pk4(o[t][h] * rl[t][h]); }
        put(2 * t + p, u32x4{op[t][2 * p][0], op[t][2 * p][1], op[t][2 * p + 1][0], op[t][2 * p + 1][1]});
      }
    };
    set2(V[0], 2, pk4(acc[0][10])); set2(V[1], 2, pk4(acc[0][11])); set2(V[4], 2, pk4(acc[0][14]));
    maxima(0);
    PIN(V[0]); PIN(V[1]); PIN(V[4]);
    weigh(0); weigh(1); SB();
    scores(1); emit(0); SB();
    set2(V[2], 2, pk4(acc[0][12])); set2(V[3], 2, pk4(acc[0][13]));
    maxima(1);
    PIN(V[2]); PIN(V[3]);
    u32x4 ex[2]; unsigned rx[2];
#pragma unroll
    for (int t = 1; t >= 0; --t) {
      ex[t] = (__builtin_bit_cast(u32x4, e[t][1]) & ac.gm[1]) | (__builtin_bit_cast(u32x4, e[t][0]) & ~ac.gm[1]);
      rx[t] = (ubits(rl[t][1]) & ac.gm[1]) | (ubits(rl[t][0]) & ~ac.gm[1]);
    }
    weigh(2); weigh(3); SB();
    if (wid == 2) {
      const float gt = rbf(__builtin_amdgcn_rcpf(1.f + __expf(-rbf(logit))));
      if (lane < 8)
        __hip_atomic_store((GAS u64*)(ws + WS_GATE + (tg * 128 + 8 * hg + lane) * 8), ((u64)(unsigned)mktag(epoch) << 32) | ubits(gt), __ATOMIC_RELAXED, __HIP_MEMORY_SCOPE_SYSTEM);
    }
    emit(1); SB();
    u32x2 ox[2];
#pragma unroll
    for (int t = 1; t >= 0; --t) {
#pragma unroll
      for (int h = 2; h < 4; ++h) { ex[t] = (__builtin_bit_cast(u32x4, e[t][h]) & ac.gm[h]) | (ex[t] & ~ac.gm[h]); rx[t] = (ubits(rl[t][h]) & ac.gm[h]) | (rx[t] & ~ac.gm[h]); }
      ox[t] = pk4(__builtin_bit_cast(f32x4, ex[t]) * fbits(rx[t]));
    }
    put(4, u32x4{ox[0][0], ox[0][1], ox[1][0], ox[1][1]});
    {
      const u32x4 c4 = {(unsigned)__builtin_amdgcn_mov_dpp((int)cs, 0x00, 0xf, 0xf, true), (unsigned)__builtin_amdgcn_mov_dpp((int)cs, 0x55, 0xf, 0xf, true),
                        (unsigned)__builtin_amdgcn_mov_dpp((int)cs, 0xaa, 0xf, 0xf, true), (unsigned)__builtin_amdgcn_mov_dpp((int)cs, 0xff, 0xf, 0xf, true)};
      if ((lane & 3) == 0) __builtin_amdgcn_raw_buffer_store_b128(c4, orsrc, lane * 4, obase + 5120, SYS);
    }
    setup4();
    bar();
    pre_read();
    asm volatile("s_waitcnt vmcnt(0)" ::: "memory");
    bar_l();
    project(IC(0));
  }
  const u64 tag = mktag(epoch);
  if (__builtin_expect(__builtin_amdgcn_ballot_w64(((unsigned)(gv[0] >> 32) ^ (unsigned)tag) | ((unsigned)(gv[1] >> 32) ^ (unsigned)tag) | ((unsigned)(gv[2] >> 32) ^ (unsigned)tag)) != 0, 0)) {
#pragma unroll
    for (int e = 0; e < 3; ++e)
#pragma unroll 1
      for (int spin = 0; spin < SPIN; ++spin) { gv[e] = __hip_atomic_load((const GAS u64*)(ws + go[e]), RLX); if (__builtin_amdgcn_ballot_w64((unsigned)(gv[e] >> 32) != (unsigned)tag) == 0) break; }
  }
  char* const out = (char*)ptr_of(ka, 6);
  auto finish = [&](auto E) AI {
    constexpr int e = E.value;
    LAS char* pr = prd[e];
    const float gt = fbits((unsigned)gv[e]), gn = rbf(1.f - gt);
    const f32x4 lo = (*(LAS f32x4*)(pr) + *(LAS f32x4*)(pr + 10240)) * gt + (*(LAS f32x4*)(pr + 20480) + *(LAS f32x4*)(pr + 30720)) * gn;
    const f32x4 hi = (*(LAS f32x4*)(pr + 5120) + *(LAS f32x4*)(pr + 15360)) * gt + (*(LAS f32x4*)(pr + 25600) + *(LAS f32x4*)(pr + 35840)) * gn;
    const u32x2 l2 = pk4(lo), h2 = pk4(hi);
    __builtin_amdgcn_raw_buffer_store_b128(u32x4{l2[0], l2[1], h2[0], h2[1]}, rsrc(out, 2048 * 1280), oo[e], 0, SYS);
  };
  if (!th) {
    __builtin_amdgcn_s_setprio(0);
    bar_l();
    finish(IC(0));
    if (wid == 0) finish(IC(1));
  }
  bar_l();
  if (wid < 5) finish(IC(2));
  if (hg == 0 && tid == 0) __hip_atomic_store((GAS u64*)(ws + WS_E + tg * 128), tag, __ATOMIC_RELAXED, __HIP_MEMORY_SCOPE_SYSTEM);
}

extern "C" void kernel_launch(void* const* d_in, const int* in_sizes, int n_in, void* d_out, int out_size, void* d_ws, size_t ws_size, hipStream_t stream) {
  (void)in_sizes; (void)n_in; (void)out_size; (void)ws_size;
  (void)hipFuncSetAttribute((const void*)fused_kernel, hipFuncAttributeMaxDynamicSharedMemorySize, LDS_BYTES);
  fused_kernel<<<256, 512, LDS_BYTES, stream>>>((const char*)d_in[0], (const char*)d_in[1], (const char*)d_in[4], (const char*)d_in[6], (const char*)d_in[2],
                                                (const char*)d_in[5], (char*)d_out, (char*)d_ws);
}
```

```cpp
#include <hip/hip_runtime.h>
#include <hip/hip_bf16.h>
#include <utility>

typedef __bf16 bf16x8 __attribute__((ext_vector_type(8)));
typedef __bf16 bf16x4 __attribute__((ext_vector_type(4)));
typedef __bf16 bf16x2 __attribute__((ext_vector_type(2)));
typedef float f32x4 __attribute__((ext_vector_type(4)));
typedef float f32x2 __attribute__((ext_vector_type(2)));
typedef unsigned u32x6 __attribute__((ext_vector_type(6)));
typedef unsigned u32x4 __attribute__((ext_vector_type(4)));
typedef unsigned u32x2 __attribute__((ext_vector_type(2)));
typedef unsigned long long u64;
#define LAS __attribute__((address_space(3)))
#define GAS __attribute__((address_space(1)))
#define DI __device__ __forceinline__
#define AI __attribute__((always_inline))
#define SB() __builtin_amdgcn_sched_barrier(0)
#define PIN(x) asm volatile("" : "+v"(x))
#define RLX __ATOMIC_RELAXED, __HIP_MEMORY_SCOPE_AGENT

constexpr int STG = 49152;
constexpr int XOFF = 30720;
constexpr int EXCH = 143360;
constexpr int GWL = EXCH + 3 * 5120;
constexpr int LDS_BYTES = 163840;
constexpr int WS_E = 0;
constexpr int WS_GATE = 4096;
constexpr int WS_O = 32768;
constexpr int REC = 5376;
constexpr int O_BYTES = 16 * 16 * 4 * REC;
constexpr int SPIN = 1 << 16;
constexpr int SYS = 17, DEV = 16;
constexpr unsigned CODE_BYTES = 28672;
#define STAMP(i)

DI void bar() { asm volatile("s_barrier" ::: "memory"); }
DI void bar_l() { __builtin_amdgcn_s_waitcnt(0xc07f); asm volatile("s_barrier" ::: "memory"); }
template <int N> DI void bar_v() { asm volatile("s_waitcnt vmcnt(%0)\n\ts_barrier" :: "i"(N) : "memory"); }
DI bf16x8 cat(bf16x4 a, bf16x4 b) { return __builtin_shufflevector(a, b, 0, 1, 2, 3, 4, 5, 6, 7); }
DI bf16x8 cat(u32x2 a, u32x2 b) { u32x4 r = {a[0], a[1], b[0], b[1]}; return __builtin_bit_cast(bf16x8, r); }
DI u32x6 cat3(u32x2 a, u32x2 b, u32x2 c) { u32x6 r = {a[0], a[1], b[0], b[1], c[0], c[1]}; return r; }
DI bf16x8 lo4(u32x6 t) { return __builtin_bit_cast(bf16x8, __builtin_shufflevector(t, t, 0, 1, 2, 3)); }
DI bf16x8 hi4(u32x6 t) { return __builtin_bit_cast(bf16x8, __builtin_shufflevector(t, t, 2, 3, 4, 5)); }
DI bf16x4 trd(LAS char* p) { return __builtin_amdgcn_ds_read_tr16_b64_v4bf16((LAS bf16x4*)p); }
DI unsigned pk2(float a, float b) { f32x2 v = {a, b}; return __builtin_bit_cast(unsigned, __builtin_convertvector(v, bf16x2)); }
DI u32x2 pk4(f32x4 v) { u32x2 r = {pk2(v[0], v[1]), pk2(v[2], v[3])}; return r; }
DI f32x4 mfma(bf16x8 a, bf16x8 b, f32x4 c) { return __builtin_amdgcn_mfma_f32_16x16x32_bf16(a, b, c, 0, 0, 0); }
DI float rbf(float v) { return __builtin_bit_cast(float, pk2(v, v) << 16); }
DI u64 mktag(u64 e) { return e * 6364136223846793005ull + 1442695040888963407ull; }
DI unsigned sum4(u32x4 v) { return v[0] + v[1] + v[2] + v[3]; }
DI float fmx(float a, float b) { return __builtin_elementwise_maximum(a, b); }
DI float fbits(unsigned u) { return __builtin_bit_cast(float, u); }
DI unsigned ubits(float f) { return __builtin_bit_cast(unsigned, f); }
template <class F, int... I> DI void sfor_(F&& f, std::integer_sequence<int, I...>) { (f(std::integral_constant<int, I>{}), ...); }
template <int N, class F> DI void sfor(F&& f) { sfor_(f, std::make_integer_sequence<int, N>{}); }
#define IC(v) std::integral_constant<int, (v)>{}

constexpr int CARRY = 4;
constexpr int PRE = 1;
constexpr int EGAP = 5;
constexpr int GATE_F = 4;
constexpr bool has_halo(int W, int f) { return f >= 5 && ((f - 5) & 3) == W; }
constexpr int grp_n(int W, int n) { return (has_halo(W, n % 15) || (W == 2 && n % 15 == GATE_F)) ? 3 : 2; }
constexpr int m_grp(int W, int m) { int n = 30 - CARRY; while (m >= grp_n(W, n % 30)) { m -= grp_n(W, n % 30); ++n; } return n - (30 - CARRY); }
constexpr int m_idx(int W, int m) { int n = 30 - CARRY; while (m >= grp_n(W, n % 30)) { m -= grp_n(W, n % 30); ++n; } return m; }
constexpr int n_mfma(int W) { int t = 0; for (int n = 0; n < 30; ++n) t += grp_n(W, n); return t; }
constexpr int n_read(int W) { return W == 2 ? 68 : 66; }
constexpr int rd_code(int W, int j) {
  constexpr int pos[2][10] = {{0, 3, 10, -1, -1, 29, 32, 41, -1, -1}, {0, 3, 8, -1, 11, 30, 33, 40, -1, 43}};
  int k = 0;
  for (int i = 0; i < 10; ++i) { const int p = pos[W == 2][i]; if (p < 0) continue; if (j == p) return 60 + i; if (j > p) ++k; }
  return j - k;
}

template <int W>
DI void gemm1(LAS char* lds, int lane, int hg, f32x4 (&acc)[2][15], float& logit) {
  const int g = lane >> 4, i16 = lane & 15, q = i16 >> 2, p = lane & 3;
  const unsigned rowb = (16 * (g >> 1) + 4 * (g & 1) + q) * 480;
  const unsigned aM = rowb + 8 * p, aX = rowb + 40 * p + 32;
  const unsigned aA = XOFF + i16 * 128 + ((g ^ (i16 >> 1)) * 16);
  const int rh = i16 < 8 ? 16 + 8 * hg + i16 : i16;
  const unsigned aH = XOFF + rh * 128 + ((g ^ ((rh >> 1) & 7)) * 16);
  f32x4 hal[3];
#pragma unroll
  for (int t = 0; t < 2; ++t)
#pragma unroll
    for (int n = 0; n < 15; ++n) acc[t][n] = f32x4{0, 0, 0, 0};
#pragma unroll
  for (int n = 0; n < 3; ++n) hal[n] = f32x4{0, 0, 0, 0};
  bf16x8 cw[CARRY], cx[3];
  const bf16x8 z8 = __builtin_bit_cast(bf16x8, u32x4{0, 0, 0, 0});
#pragma unroll
  for (int n = 0; n < CARRY; ++n) cw[n] = z8;
#pragma unroll
  for (int n = 0; n < 3; ++n) cx[n] = z8;
  auto one = [&](int f, int i, bf16x8 wf, bf16x8 xf) AI {
    f32x4& d = i < 2 ? acc[i][f] : hal[(f - 5) >> 2];
    d = f < 10 ? mfma(wf, xf, d) : mfma(xf, wf, d);
  };
  constexpr int NM = n_mfma(W), NR = n_read(W), NP = W == 2 ? 7 : 6;
  unsigned buf = 0;
  LAS char* pp[7] = {lds + aM, lds + aX, lds + aA, lds + (aA ^ 64), lds + aH, lds + (aH ^ 64), lds + (GWL + g * 16)};
  int it = 10, dl = STG;
#pragma unroll 1
  do {
    bf16x4 wh[60]; bf16x8 xf[10];
    auto rd = [&](auto J) AI {
      constexpr int c = rd_code(W, J.value);
      if constexpr (c < 60) {
        constexpr int n = c >> 1, ks = n / 15, f = n % 15, sec = f / 5, jj = f % 5;
        wh[c] = trd((jj < 4 ? pp[0] + 40 * jj : pp[1]) + (ks * 32 + (c & 1) * 8) * 480 + sec * 160);
      } else {
        constexpr int ks = (c - 60) / 5, t = (c - 60) % 5;
        xf[c - 60] = *(LAS bf16x8*)(t < 2 ? pp[2 + ks] + (2 * W + 1 + t) * 2048 : t == 2 ? pp[4 + ks] : pp[6] + ks * 64);
      }
    };
    sfor<NM>([&](auto M) AI {
      constexpr int m = M.value, gi = m_grp(W, m), i0 = m_idx(W, m), i = (i0 < 2 && (gi & 1)) ? 1 - i0 : i0;
      if constexpr (m == PRE) { bar_l(); SB(); }
      if constexpr (gi < CARRY) one(15 - CARRY + gi, i, cw[gi], cx[i]);
      else {
        constexpr int n = gi - CARRY, ks = n / 15;
        if constexpr (W == 2 && n % 15 == GATE_F && i == 2) hal[2] = mfma(xf[ks * 5 + 4], xf[ks * 5 + 2], hal[2]);
        else one(n % 15, i, cat(wh[2 * n], wh[2 * n + 1]), xf[ks * 5 + i]);
      }
      SB();
      constexpr int D2 = NR - (NM - EGAP), j = m - PRE;
      if constexpr (j >= 0 && j < D2) { rd(IC(2 * j)); rd(IC(2 * j + 1)); }
      else if constexpr (j >= 0 && D2 + j < NR) rd(IC(D2 + j));
      else if constexpr (j >= 0) {
        constexpr int u = m - (NM - (EGAP - PRE)), NG = EGAP - PRE, PER = (NP + NG - 1) / NG;
        sfor<PER>([&](auto K) AI { constexpr int k = u * PER + K.value; if constexpr (k < NP) { pp[k] += (k == 6 ? 128 : dl); PIN(pp[k]); } });
      }
      if constexpr (m == 21) { dl = (buf == 2 * STG) ? -2 * STG : STG; asm volatile("" : "+s"(dl)); }
      if constexpr (m == 23) { buf += dl; --it; asm volatile("" : "+s"(buf), "+s"(it)); }
      SB();
    });
#pragma unroll
    for (int n = 0; n < CARRY; ++n) cw[n] = cat(wh[2 * (30 - CARRY + n)], wh[2 * (30 - CARRY + n) + 1]);
#pragma unroll
    for (int n = 0; n < 3; ++n) cx[n] = xf[5 + n];
  } while (it);
#pragma unroll
  for (int gi = 0; gi < CARRY; ++gi)
#pragma unroll
    for (int i = 0; i < 3; ++i)
      if (i < grp_n(W, 30 - CARRY + gi)) one(15 - CARRY + gi, i, cw[gi], cx[i]);
#pragma unroll
  for (int f = 5; f < 15; ++f)
    if (has_halo(W, f)) *(LAS u32x2*)(lds + EXCH + (f - 5) * 512 + lane * 8) = pk4(hal[(f - 5) >> 2]);
  if (W == 2) logit = hal[2][0];
}

struct AttnConst { f32x4 biasC, biasP, biasP0; unsigned selC[2], selP[2], gm[4]; };
DI bf16x8 q_operand(int h, u32x2 qh, u32x2 qx, unsigned gm) { const u32x2 qm = {qx[0] & gm, qx[1] & gm}; return (h & 1) ? cat(qm, qh) : cat(qh, qm); }
DI bf16x8 k_operand(int h, const u32x6 (&k)[2]) { return (h & 1) ? hi4(k[h >> 1]) : lo4(k[h >> 1]); }
#define PW "s_waitcnt vmcnt(3)\n\t"
#define DMA(v, srd, so, mo, si, mi) "s_add_u32 m0, " mo ", " #mi "\n\ts_add_u32 %0, " so ", " #si "\n\tbuffer_load_dwordx4 " v ", " srd ", %0 offen lds\n\t"

typedef unsigned u32x16 __attribute__((ext_vector_type(16)));
DI const char* ptr_of(u32x16 k, int i) { return (const char*)(((u64)k[2 * i + 1] << 32) | k[2 * i]); }
DI __amdgpu_buffer_rsrc_t rsrc(const char* p, int bytes) { return __builtin_amdgcn_make_buffer_rsrc((void*)p, 0, bytes, 0x00020000); }

extern "C" __global__ __launch_bounds__(512, 2) void fused_kernel(const char*, const char*, const char*, const char*, const char*, const char*, char*, char*) {
  extern __shared__ __attribute__((aligned(16))) char smem[];
  u32x16 ka;
  asm volatile("s_load_dwordx16 %0, %1, 0x0" : "=&s"(ka) : "s"(__builtin_amdgcn_kernarg_segment_ptr()));
  LAS char* lds = (LAS char*)smem;
  const unsigned ldsb = (unsigned)(size_t)lds;
  const int tid = threadIdx.x, lane = tid & 63;
  const int wid = __builtin_amdgcn_readfirstlane(tid >> 6);
  const int b = blockIdx.x, xcd = b & 7, r = b >> 3;
  const int tg = (xcd & 1) * 8 + (r & 7), hg = (xcd >> 1) * 4 + (r >> 3);
  const int par = tg >> 3, ch = tg & 7;
  const int kq = wid & 3, th = wid >> 2;
#define CNT(i) (void)0
  STAMP(0);
  if (wid == 2) {
    const char* pc; unsigned back;
    asm volatile("s_getpc_b64 %0\nfk_here:\n\ts_mov_b32 %1, fk_here-fused_kernel" : "=s"(pc), "=s"(back));
    pc -= back;
    const unsigned po = (r * 8 + (lane & 7)) * 128;
    if (lane < 8 && po < CODE_BYTES) asm volatile("s_mov_b32 m0, %2\n\ts_nop 0\n\tglobal_load_lds_dword %0, %1" :: "v"(po), "s"(pc), "s"(ldsb + XOFF) : "memory");
  }
  const int stg0 = (((5 * ((ch + hg) & 3) + 1) >> 1) + par + ((hg >> 2) & 1)) % 10;
  const int l30 = lane >= 30 ? lane - 30 : lane, sec = (l30 >= 10) + (l30 >= 20);
  unsigned vW = (lane >= 30 ? 3840 : 0) + sec * (1280 - 160) + l30 * 16;
  unsigned vX0 = (lane >> 3) * 2560 + (((lane & 7) ^ (lane >> 4)) * 16), vX1 = vX0 ^ 64;
  unsigned vH = (lane >> 3) * 2560 + (((lane & 7) ^ (4 + kq)) * 16);
  unsigned oW = kq * 16 * 3840 + (hg & 7) * 160;
  unsigned oX = (2 * (128 * ch + 32 * kq) + par) * 1280;
  unsigned oH = (2 * (128 * ch + 2 * kq - (ch ? 8 : 0)) + par) * 1280;
  unsigned sW0 = oW + stg0 * (64 * 3840), sX0 = oX + stg0 * 128, sH0 = oH + stg0 * 128;
  unsigned dW = ldsb + kq * 7680, dX = ldsb + XOFF + (16 + 32 * kq) * 128, dH = ldsb + XOFF + (8 + 2 * kq) * 128;
  asm volatile("s_waitcnt lgkmcnt(0)" : "+s"(ka), "+v"(vW), "+v"(vX0), "+v"(vX1), "+v"(vH), "+s"(sW0), "+s"(sX0), "+s"(sH0), "+s"(dW), "+s"(dX), "+s"(dH));
  const char* const x = ptr_of(ka, 0);
  char* const ws = (char*)ptr_of(ka, 7);
  u64 epoch, epoch_in;
  asm volatile("s_load_dwordx2 %0, %1, 0x0" : "=&s"(epoch_in) : "s"(ws + WS_E + tg * 128) : "memory");

  const auto wsrd = rsrc(hg >= 8 ? ptr_of(ka, 2) : ptr_of(ka, 1), 640 * 3840), xsrd = rsrc(x, 2048 * 1280);
  auto partA = [&](unsigned sw_, unsigned dw) AI {
    unsigned t;
    asm volatile(
      "s_mov_b32 exec_hi, 0x0fffffff\n\t"
      DMA("%1", "%2", "%3", "%4", 0, 0) DMA("%1", "%2", "%3", "%4", 7680, 960) DMA("%1", "%2", "%3", "%4", 30720, 1920)
      DMA("%1", "%2", "%3", "%4", 38400, 2880) DMA("%1", "%2", "%3", "%4", 15360, 3840) DMA("%1", "%2", "%3", "%4", 23040, 4800)
      "s_mov_b32 exec_hi, -1"
      : "=&s"(t) : "v"(vW), "s"(wsrd), "s"(sw_), "s"(dw) : "scc", "memory");
  };
  auto partB = [&](unsigned sw_, unsigned sx, unsigned sh, unsigned dw, unsigned dx, unsigned dh) AI {
    unsigned t;
    asm volatile(
      DMA("%2", "%6", "%8", "%11", 0, 0) DMA("%3", "%6", "%8", "%11", 20480, 1024) DMA("%2", "%6", "%8", "%11", 40960, 2048) DMA("%3", "%6", "%8", "%11", 61440, 3072)
      "s_mov_b32 exec_hi, 0x0fffffff\n\t"
      DMA("%1", "%5", "%7", "%10", 46080, 5760) DMA("%1", "%5", "%7", "%10", 53760, 6720)
      "s_mov_b64 exec, 0xffff\n\t"
      DMA("%4", "%6", "%9", "%12", 0, 0)
      "s_mov_b64 exec, -1"
      : "=&s"(t)
      : "v"(vW), "v"(vX0), "v"(vX1), "v"(vH), "s"(wsrd), "s"(xsrd), "s"(sw_), "s"(sx), "s"(sh), "s"(dw), "s"(dx), "s"(dh)
      : "scc", "memory");
  };
  if (wid >= 4) {
    STAMP(14);
    partB(sW0, sX0, sH0, dW, dX, dH); partA(sW0, dW);
    if (kq < 2) {
      const int c = kq * 64 + lane, sg = stg0 + (c >> 3);
      unsigned t;
      asm volatile(DMA("%1", "%2", "0", "%3", 0, 0) : "=&s"(t)
                   : "v"(c < 80 ? (sg >= 10 ? sg - 10 : sg) * 128 + (c & 7) * 16 : 2048), "s"(rsrc(ptr_of(ka, 3), 1280)), "s"(ldsb + GWL + kq * 1024) : "scc", "memory");
    }
    partA(stg0 == 9 ? oW : sW0 + 64 * 3840, dW + STG);
    STAMP(9);
  }
  SB();
  const int g = lane >> 4, i16 = lane & 15;
  const int hf = (hg >> 2) & 1, ct = (hg & 3) + 4 * (hg >> 3);
  unsigned so[4][3];
  LAS char *pw, *pw3;
  auto setup3 = [&]() AI {
    int tq = tg; asm volatile("" : "+s"(tq));
#pragma unroll
    for (int i = 0; i < 4; ++i) {
      const unsigned ob = ((tq * 16 + 4 * kq + ((tq + i) & 3)) * 4 + 2 * hf + th) * REC;
      so[i][0] = ob + (tq & 4) * 256; so[i][1] = ob + ((tq & 4) ^ 4) * 256; so[i][2] = ob + 4096;
      asm volatile("" : "+s"(so[i][0]), "+s"(so[i][1]), "+s"(so[i][2]));
    }
  };
  unsigned oo[3], go[3]; LAS char *pwr, *prd[3];
  auto setup4 = [&]() AI {
    int td = tid; PIN(td);
#pragma unroll
    for (int e = 0; e < 3; ++e) {
      const int c = e == 1 ? 256 + (td & 63) : td < 320 ? td : 0, tk = c / 10, tok = tk + (e == 2 ? 32 : 0), j = c - tk * 10;
      oo[e] = (2 * (128 * ch + 64 * hf + tok) + par) * 1280 + ct * 160 + j * 16; PIN(oo[e]);
      go[e] = WS_GATE + (tg * 128 + 64 * hf + tok) * 8; PIN(go[e]);
      prd[e] = lds + ((e == 2 ? 0 : 122880) + (c ^ ((tk >> 2) & 1)) * 16); PIN(prd[e]);
    }
    pwr = lds + ((th ? 0 : 122880) + kq * 10240 + ((td >> 4) & 1) * 5120 + (td & 15) * 160 + (((td >> 5) ^ (td >> 2)) & 1) * 16);
    PIN(pwr);
  };
  bf16x8 pre[4][5];
  auto pre_read = [&]() AI {
    int ln = lane; PIN(ln);
    pw = lds + ((ln >> 2) * 160 + 8 * (ln & 3) + kq * 5120); pw3 = pw + 3 * 20480;
    PIN(pw); PIN(pw3);
#pragma unroll
    for (int ps = 0; ps < 4; ++ps)
#pragma unroll
      for (int c5 = 0; c5 < 5; ++c5) pre[ps][c5] = cat(trd(pw3 + ps * 20480 + c5 * 32), trd(pw3 + ps * 20480 + c5 * 32 + 2560));
  };
  u32x4 late[3][5];

  u64 gv[3];
  auto project = [&](auto THC) AI {
    constexpr bool TH = THC.value;
    STAMP(4);
    const u64 tag = mktag(epoch);
    const auto orsrc = rsrc(ws + WS_O, O_BYTES);
    struct Rec { u32x4 m[2][2], x; unsigned cs; };
    Rec rec[4];
    const unsigned l16 = lane * 16, l4 = lane * 4;
    auto ldrec = [&](int i, int j) AI {
      if (j < 4) rec[i].m[j >> 1][j & 1] = __builtin_amdgcn_raw_buffer_load_b128(orsrc, l16 + (j & 1) * 2048, so[i][j >> 1], DEV);
      else if (j == 4) rec[i].x = __builtin_amdgcn_raw_buffer_load_b128(orsrc, l16, so[i][2], DEV);
      else rec[i].cs = __builtin_amdgcn_raw_buffer_load_b32(orsrc, l4 + 1024, so[i][2], DEV);
    };
    auto reload = [&](int i) AI {
      Rec& q = rec[i];
      asm volatile("buffer_load_dwordx4 %0, %6, %8, %9 offen sc1\n\tbuffer_load_dwordx4 %1, %6, %8, %9 offen offset:2048 sc1\n\t"
                   "buffer_load_dwordx4 %2, %6, %8, %10 offen sc1\n\tbuffer_load_dwordx4 %3, %6, %8, %10 offen offset:2048 sc1\n\t"
                   "buffer_load_dwordx4 %4, %6, %8, %11 offen sc1\n\tbuffer_load_dword %5, %7, %8, %11 offen offset:1024 sc1\n\ts_waitcnt vmcnt(0)"
                   : "+v"(q.m[0][0]), "+v"(q.m[0][1]), "+v"(q.m[1][0]), "+v"(q.m[1][1]), "+v"(q.x), "+v"(q.cs)
                   : "v"(l16), "v"(l4), "s"(orsrc), "s"(so[i][0]), "s"(so[i][1]), "s"(so[i][2]) : "memory");
    };
    auto word = [&](int i, int k) AI { return k < 16 ? rec[i].m[k >> 3][(k >> 2) & 1][k & 3] : rec[i].x[k & 3]; };
    auto stale = [&](int i) AI {
      unsigned c = (unsigned)tag;
#pragma unroll
      for (int k = 0; k < 20; ++k) c += word(i, k);
      return __builtin_amdgcn_ballot_w64(c != rec[i].cs);
    };
    auto refetch = [&](int i, int n) AI {
#pragma unroll 1
      for (int spin = 0; spin < SPIN; ++spin) { CNT(i); reload(i); if (!stale(i)) break; }
#pragma unroll
      for (int k = 1; k < n; ++k) reload(i + k);
    };
    __builtin_amdgcn_s_setprio(TH ? 1 : 2);
    if constexpr (TH) {
      LAS char* lp = lds + 3 * 20480 + kq * 5120 + lane * 16;
#pragma unroll
      for (int ps = 0; ps < 3; ++ps)
#pragma unroll
        for (int i = 0; i < 5; ++i) *(LAS u32x4*)(lp + ((ps + 2) % 3) * 20480 + i * 1024) = late[(ps + 2) % 3][i];
      SB(); setup3(); setup4();
      bar_l();
    }
    SB();
    sfor<12>([&](auto J) AI { ldrec(J.value / 6, J.value % 6); SB(); });
    STAMP(6);
    SB();
    bf16x4 wq[10][10];
    auto rdq = [&](auto P, auto I) AI {
      constexpr int G[10] = {3, 4, 5, 6, 0, 1, 2, 3, 4, 5};
      wq[P.value][I.value] = trd((G[P.value] < 3 ? pw + G[P.value] * 20480 : pw3 + (G[P.value] - 3) * 20480) + (I.value >> 1) * 32 + (I.value & 1) * 2560);
    };
    SB();
    if (__builtin_expect(stale(0) != 0, 0)) refetch(0, 2);
    if constexpr (TH) { if (__builtin_expect(stale(1) != 0, 0)) refetch(1, 1); }
    STAMP(5);
    f32x4 a3[2][5];
    unsigned csum = 0; u64 bad = 0; bf16x8 xo[2];
    sfor<100>([&](auto M) AI {
      constexpr int m = M.value;
      constexpr int pos = m < 70 ? m / 10 : 7 + ((m - 70) % 6) / 2, c5 = m < 70 ? (m % 10) >> 1 : (m - 70) / 6, t = m & 1;
      constexpr int pp = pos < 4 ? pos : pos - 1;
      if constexpr ((m == 20 && !TH) || m == 50 || m == 70) { constexpr int i = m == 20 ? 1 : m == 50 ? 2 : 3; if (__builtin_expect(bad != 0, 0)) refetch(i, i == 1 ? 1 : 4 - i); SB(); }
      if constexpr (m == 80 && TH) { bar(); SB(); }
      const bf16x8 ov = (pos == 4 || pos == 9) ? xo[t] : __builtin_bit_cast(bf16x8, rec[pp >> 1].m[pp & 1][t]);
      const bf16x8 wv = pos < 4 ? pre[pos & 3][c5] : cat(wq[pos][2 * c5], wq[pos][2 * c5 + 1]);
      if constexpr (m < 10) asm volatile("v_mfma_f32_16x16x32_bf16 %0, %1, %2, 0" : "=&v"(a3[t][c5]) : "v"(wv), "v"(ov));
      else asm volatile("v_mfma_f32_16x16x32_bf16 %0, %1, %2, %0" : "+v"(a3[t][c5]) : "v"(wv), "v"(ov));
      SB();
      if constexpr (m < 12) ldrec(2 + m / 6, m % 6);
      if constexpr (!TH && m == 12) bar();
      if constexpr (!TH && m > 12 && m < 16) gv[m - 13] = __hip_atomic_load((const GAS u64*)(ws + go[m - 13]), RLX);
      if constexpr (TH && m == 12) { gv[2] = __hip_atomic_load((const GAS u64*)(ws + go[2]), RLX); gv[0] = gv[1] = gv[2]; }
      if constexpr (m >= 20 && m < 50) rdq(IC(m / 10 + 2), IC(m % 10));
      if constexpr (m >= 50 && m < 80) rdq(IC(7 + ((m - 50) % 6) / 2), IC(2 * ((m - 50) / 6) + (m & 1)));
      constexpr int ci = m < 20 ? 1 : m < 50 ? 2 : 3, cb = m < 20 ? 8 : m < 50 ? 36 : 56, k = m - cb;
      if constexpr (m < 70 && (m >= 20 || !TH) && k >= 0 && k < 10) { csum = (k ? csum : (unsigned)tag) + word(ci, 2 * k) + word(ci, 2 * k + 1); PIN(csum); }
      if constexpr (m < 70 && (m >= 20 || !TH) && k == 10) { bad = __builtin_amdgcn_ballot_w64(csum != rec[ci].cs); asm volatile("" : "+s"(bad)); }
      if constexpr (m == 30 || m == 31 || m == 70 || m == 71) {
        constexpr int r0 = m < 70 ? 0 : 2;
        xo[t] = cat(u32x2{rec[r0].x[2 * t], rec[r0].x[2 * t + 1]}, u32x2{rec[r0 + 1].x[2 * t], rec[r0 + 1].x[2 * t + 1]}); PIN(xo[t]);
      }
      if constexpr (m >= 82 && (m - 82) % 6 < 2) *(LAS f32x4*)(pwr + t * 16 * 160 + ((m - 82) / 6) * 32) = a3[t][(m - 82) / 6];
      SB();
    });
    *(LAS f32x4*)(pwr + 3 * 32) = a3[0][3];
    asm volatile("s_nop 1" : "+v"(a3[1][3]));
    *(LAS f32x4*)(pwr + 16 * 160 + 3 * 32) = a3[1][3];
    asm volatile("s_nop 5" : "+v"(a3[0][4]), "+v"(a3[1][4]));
    *(LAS f32x4*)(pwr + 4 * 32) = a3[0][4];
    *(LAS f32x4*)(pwr + 16 * 160 + 4 * 32) = a3[1][4];
    STAMP(7);
  };

  if (__builtin_expect(wid >= 4, 0)) {
    int s1 = stg0 == 9 ? 0 : stg0 + 1, s2 = stg0 >= 8 ? stg0 - 8 : stg0 + 2;
    const auto wosrd = rsrc(kq >> 1 ? ptr_of(ka, 5) : ptr_of(ka, 4), 640 * 1280);
    const int rot = tg & 3, sw = (tg >> 2) & 1;
    unsigned vm[5], ve[5];
#pragma unroll
    for (int i = 0; i < 5; ++i) {
      const int c = i * 64 + lane, rr = c / 10, wi = c - rr * 10;
      vm[i] = (20 * (rr >> 4) + (rr & 15)) * 1280 + wi * 16; PIN(vm[i]);
      ve[i] = (20 * ((rr >> 2) & 3) + 16 + (rr & 3)) * 1280 + wi * 16; PIN(ve[i]);
    }
    auto wo_off = [&](int pos, unsigned (&v)[5], unsigned& sq) AI {
      if (pos == 4 || pos == 9) {
        const unsigned ha = (4 * (kq & 1) + ((rot + (pos == 9 ? 2 : 0)) & 3)) * 102400, hb = (4 * (kq & 1) + ((rot + (pos == 9 ? 3 : 1)) & 3)) * 102400;
#pragma unroll
        for (int i = 0; i < 5; ++i) v[i] = ve[i] + ((i * 64 + lane >= 160) ? hb : ha);
        sq = ct * 160;
      } else {
        const int pp = pos < 4 ? pos : pos - 1;
#pragma unroll
        for (int i = 0; i < 5; ++i) v[i] = vm[i];
        sq = ct * 160 + (4 * (kq & 1) + ((rot + (pp >> 1)) & 3)) * 102400 + ((pp & 1) ^ sw) * 51200;
      }
    };
    auto wo_pos = [&](int pos, int grp, bool paced) AI {
      unsigned v[5], sq, t;
      wo_off(pos, v, sq);
#define WO5(P) asm volatile(P DMA("%1", "%6", "%7", "%8", 0, 0) P DMA("%2", "%6", "%7", "%8", 0, 1024) P DMA("%3", "%6", "%7", "%8", 0, 2048) P DMA("%4", "%6", "%7", "%8", 0, 3072) P DMA("%5", "%6", "%7", "%8", 0, 4096) \
                   : "=&s"(t) : "v"(v[0]), "v"(v[1]), "v"(v[2]), "v"(v[3]), "v"(v[4]), "s"(wosrd), "s"(sq), "s"(ldsb + grp * 20480 + kq * 5120) : "scc", "memory")
      if (paced) WO5(PW); else WO5("");
    };
    auto late_load = [&](int ps, bool paced) AI {
      unsigned v[5], sq;
      wo_off(7 + ps, v, sq);
#pragma unroll
      for (int i = 0; i < 5; ++i) { if (paced) asm volatile(PW ::: "memory"); late[ps][i] = __builtin_amdgcn_raw_buffer_load_b128(wosrd, v[i], sq, 0); }
    };
    unsigned b1 = STG, b2 = 2 * STG;
    unsigned q0, q1, q2, q3, q4, q5, q6, q7;
    auto prep = [&]() AI {
      q0 = oW + s1 * (64 * 3840); q1 = oX + s1 * 128; q2 = oH + s1 * 128; q3 = dW + b1; q4 = dX + b1; q5 = dH + b1; q6 = oW + s2 * (64 * 3840); q7 = dW + b2;
      asm volatile("" : "+s"(q0), "+s"(q1), "+s"(q2), "+s"(q3), "+s"(q4), "+s"(q5), "+s"(q6), "+s"(q7));
      s1 = s2; s2 = (s2 == 9) ? 0 : s2 + 1;
      b1 = b2; b2 = (b2 == 2 * STG) ? 0 : b2 + STG;
    };
    prep();
    asm volatile("s_waitcnt vmcnt(6) lgkmcnt(0)\n\ts_mov_b64 %0, %1" : "=s"(epoch) : "s"(epoch_in) : "memory");
#pragma unroll 1
    for (int it = 0; it < 8; ++it) {
      SB(); bar(); SB();
      partB(q0, q1, q2, q3, q4, q5); partA(q6, q7);
      SB();
      prep();
      asm volatile("s_waitcnt vmcnt(6)" ::: "memory");
    }
    bar();
    STAMP(19);
    partB(q0, q1, q2, q3, q4, q5);
    wo_pos(0, 3, false);
    bar_v<5>();
    STAMP(13);
    wo_pos(1, 4, false); wo_pos(2, 5, false); wo_pos(3, 6, false);
    late_load(2, false);
    STAMP(1);
    bar();
    STAMP(2);
    wo_pos(4, 0, false); wo_pos(5, 1, true); wo_pos(6, 2, true);
    late_load(0, true);
    STAMP(3);
    bar();
    late_load(1, false);
    pre_read();
    __builtin_amdgcn_s_waitcnt(0xc07f);
    bar_v<10>();
    project(IC(1));
  } else {
    const float NINF = -__builtin_inff();
    AttnConst ac;
    const bool first = (ch == 0) && (wid == 0);
#pragma unroll
    for (int j = 0; j < 4; ++j) {
      const int c = 4 * g + j;
      ac.biasC[j] = (c <= i16 && c >= i16 - 7) ? 0.f : NINF;
      ac.biasP[j] = (c >= i16 + 9) ? 0.f : NINF;
      ac.biasP0[j] = first ? NINF : ac.biasP[j];
      ac.gm[j] = (g == j) ? 0xffffffffu : 0u; PIN(ac.gm[j]);
    }
#pragma unroll
    for (int j = 0; j < 2; ++j) {
      const bool c0 = 4 * g + 2 * j <= i16, c1 = 4 * g + 2 * j + 1 <= i16;
      ac.selC[j] = (c0 ? 0x0302u : 0x0c0cu) | (c1 ? 0x07060000u : 0x0c0c0000u); PIN(ac.selC[j]);
      ac.selP[j] = (c0 ? 0x0c0cu : 0x0302u) | (c1 ? 0x0c0c0000u : 0x07060000u); PIN(ac.selP[j]);
    }
    PIN(ac.biasC); PIN(ac.biasP); PIN(ac.biasP0);
    setup3();
    f32x4 acc[2][15]; float logit = 0.f;
    asm volatile("s_waitcnt lgkmcnt(0)\n\ts_mov_b64 %0, %1" : "=s"(epoch) : "s"(epoch_in) : "memory");
    STAMP(9);
    if (wid == 0) gemm1<0>(lds, lane, hg, acc, logit);
    else if (wid == 1) gemm1<1>(lds, lane, hg, acc, logit);
    else if (wid == 2) gemm1<2>(lds, lane, hg, acc, logit);
    else gemm1<3>(lds, lane, hg, acc, logit);
    asm volatile("s_waitcnt vmcnt(0)" ::: "memory");
    STAMP(1);
    const auto orsrc = rsrc(ws + WS_O, O_BYTES);
    u32x6 K[3][2], V[5];
    auto set2 = [&](u32x6& t, int i, u32x2 v) AI { t[i] = v[0]; t[i + 1] = v[1]; };
    K[2][0] = cat3(pk4(acc[1][5]), pk4(acc[1][9]), pk4(acc[1][6])); PIN(K[2][0]);
    K[2][1] = cat3(pk4(acc[1][7]), u32x2{K[2][0][2], K[2][0][3]}, pk4(acc[1][8])); PIN(K[2][1]);
#pragma unroll
    for (int f = 0; f < 5; ++f) { asm volatile("" : "=v"(V[f])); set2(V[f], 4, pk4(acc[1][10 + f])); PIN(V[f]); }
    if (wid < 3) {
      LAS char* e = lds + EXCH + (wid + 1) * 5120 + lane * 8;
      *(LAS u32x2*)(e) = u32x2{K[2][0][0], K[2][0][1]}; *(LAS u32x2*)(e + 512) = u32x2{K[2][0][4], K[2][0][5]};
      *(LAS u32x2*)(e + 1024) = u32x2{K[2][1][0], K[2][1][1]}; *(LAS u32x2*)(e + 1536) = u32x2{K[2][1][4], K[2][1][5]};
      *(LAS u32x2*)(e + 2048) = u32x2{K[2][0][2], K[2][0][3]};
#pragma unroll
      for (int f = 0; f < 5; ++f) *(LAS u32x2*)(e + (5 + f) * 512) = u32x2{V[f][4], V[f][5]};
    }
    SB();
    const u32x2 qx[2] = {pk4(acc[0][4]), pk4(acc[1][4])};
    bf16x8 qb[2][4]; f32x4 sc[2][4], sp[2][4];
    auto scores = [&](int p) AI {
      K[1][p] = cat3(pk4(acc[0][5 + 2 * p]), p ? u32x2{K[1][0][2], K[1][0][3]} : pk4(acc[0][9]), pk4(acc[0][6 + 2 * p])); PIN(K[1][p]);
#pragma unroll
      for (int t = 1; t >= 0; --t)
#pragma unroll
        for (int h = 2 * p; h < 2 * p + 2; ++h) {
          qb[t][h] = q_operand(h, pk4(acc[t][h]), qx[t], ac.gm[h]);
          sc[t][h] = mfma(k_operand(h, K[t + 1]), qb[t][h], ac.biasC);
          if (t) sp[t][h] = mfma(k_operand(h, K[t]), qb[t][h], ac.biasP);
        }
    };
    scores(0);
    SB();
    bar_l();
    STAMP(2);
    SB();
    {
      LAS char* e = lds + EXCH + wid * 5120 + lane * 8;
      K[0][0] = cat3(*(volatile LAS u32x2*)(e), *(volatile LAS u32x2*)(e + 2048), *(volatile LAS u32x2*)(e + 512));
      set2(V[0], 0, *(volatile LAS u32x2*)(e + 5 * 512)); set2(V[1], 0, *(volatile LAS u32x2*)(e + 6 * 512)); set2(V[4], 0, *(volatile LAS u32x2*)(e + 9 * 512));
      K[0][1] = cat3(*(volatile LAS u32x2*)(e + 1024), *(volatile LAS u32x2*)(e + 2048), *(volatile LAS u32x2*)(e + 1536));
      set2(V[2], 0, *(volatile LAS u32x2*)(e + 7 * 512)); set2(V[3], 0, *(volatile LAS u32x2*)(e + 8 * 512));
    }
    const unsigned obase = ((tg * 16 + hg) * 4 + wid) * REC;
    unsigned cs = (unsigned)mktag(epoch);
    auto put = [&](int k, u32x4 v) AI {
      __builtin_amdgcn_raw_buffer_store_b128(v, orsrc, lane * 16 + (k & 3) * 1024, obase + (k >> 2) * 4096, SYS);
      cs += sum4(v);
    };
    const float CS = 0.22360679774997896f * 1.4426950408889634f;
    const bf16x8 ones = __builtin_bit_cast(bf16x8, u32x4{0x3f803f80u, 0x3f803f80u, 0x3f803f80u, 0x3f803f80u});
    const f32x4 z4 = {0, 0, 0, 0};
    f32x4 s[2][4], l[2][4], o[2][4], e[2][4]; float mc[2][4], rl[2][4]; u32x2 op[2][4];
    auto maxima = [&](int p) AI {
      unsigned ml[4];
#pragma unroll
      for (int h = 2 * p; h < 2 * p + 2; ++h) { if (h == 2 * p) PIN(K[0][p]); sp[0][h] = mfma(k_operand(h, K[0]), qb[0][h], ac.biasP0); }
#pragma unroll
      for (int u = 3; u >= 0; --u) {
        const int t = u >> 1, h = 2 * p + (u & 1);
        s[t][h] = __builtin_elementwise_maximum(sc[t][h], sp[t][h]);
        ml[u] = ubits(fmx(fmx(fmx(s[t][h][0], s[t][h][1]), s[t][h][2]), s[t][h][3]));
      }
      auto r2 = __builtin_amdgcn_permlane16_swap(ml[2], ml[3], false, false);
      auto r1 = __builtin_amdgcn_permlane16_swap(ml[0], ml[1], false, false);
      auto r3 = __builtin_amdgcn_permlane32_swap(ubits(fmx(fbits(r1[0]), fbits(r1[1]))), ubits(fmx(fbits(r2[0]), fbits(r2[1]))), false, false);
      const unsigned w = ubits(fmx(fbits(r3[0]), fbits(r3[1])) * -CS);
      auto r4 = __builtin_amdgcn_permlane32_swap(w, w, false, false);
      auto r5 = __builtin_amdgcn_permlane16_swap(r4[0], r4[0], false, false);
      auto r6 = __builtin_amdgcn_permlane16_swap(r4[1], r4[1], false, false);
      mc[0][2 * p] = fbits(r5[0]); mc[0][2 * p + 1] = fbits(r5[1]); mc[1][2 * p] = fbits(r6[0]); mc[1][2 * p + 1] = fbits(r6[1]);
    };
    auto weigh = [&](int h) AI {
#pragma unroll
      for (int t = 1; t >= 0; --t) {
        const f32x4 a = __builtin_elementwise_fma(s[t][h], f32x4{CS, CS, CS, CS}, f32x4{mc[t][h], mc[t][h], mc[t][h], mc[t][h]});
        unsigned pr[4];
#pragma unroll
        for (int j = 0; j < 4; ++j) pr[j] = ubits(__builtin_amdgcn_exp2f(a[j]));
        const bf16x8 pb = cat(u32x2{__builtin_amdgcn_perm(pr[1], pr[0], ac.selP[0]), __builtin_amdgcn_perm(pr[3], pr[2], ac.selP[1])},
                              u32x2{__builtin_amdgcn_perm(pr[1], pr[0], ac.selC[0]), __builtin_amdgcn_perm(pr[3], pr[2], ac.selC[1])});
        l[t][h] = mfma(ones, pb, z4);
        o[t][h] = mfma(t ? hi4(V[h]) : lo4(V[h]), pb, z4);
        e[t][h] = mfma(t ? hi4(V[4]) : lo4(V[4]), pb, z4);
      }
    };
    auto emit = [&](int p) AI {
#pragma unroll
      for (int t = 1; t >= 0; --t) {
#pragma unroll
        for (int h = 2 * p; h < 2 * p + 2; ++h) { rl[t][h] = __builtin_amdgcn_rcpf(l[t][h][0]); op[t][h] = pk4(o[t][h] * rl[t][h]); }
        put(2 * t + p, u32x4{op[t][2 * p][0], op[t][2 * p][1], op[t][2 * p + 1][0], op[t][2 * p + 1][1]});
      }
    };
    set2(V[0], 2, pk4(acc[0][10])); set2(V[1], 2, pk4(acc[0][11])); set2(V[4], 2, pk4(acc[0][14]));
    maxima(0);
    PIN(V[0]); PIN(V[1]); PIN(V[4]);
    weigh(0); weigh(1); SB();
    scores(1); emit(0); SB();
    set2(V[2], 2, pk4(acc[0][12])); set2(V[3], 2, pk4(acc[0][13]));
    maxima(1);
    PIN(V[2]); PIN(V[3]);
    u32x4 ex[2]; unsigned rx[2];
#pragma unroll
    for (int t = 1; t >= 0; --t) {
      ex[t] = (__builtin_bit_cast(u32x4, e[t][1]) & ac.gm[1]) | (__builtin_bit_cast(u32x4, e[t][0]) & ~ac.gm[1]);
      rx[t] = (ubits(rl[t][1]) & ac.gm[1]) | (ubits(rl[t][0]) & ~ac.gm[1]);
    }
    weigh(2); weigh(3); SB();
    if (wid == 2) {
      const float gt = rbf(__builtin_amdgcn_rcpf(1.f + __expf(-rbf(logit))));
      if (lane < 8)
        __hip_atomic_store((GAS u64*)(ws + WS_GATE + (tg * 128 + 8 * hg + lane) * 8), ((u64)(unsigned)mktag(epoch) << 32) | ubits(gt), __ATOMIC_RELAXED, __HIP_MEMORY_SCOPE_SYSTEM);
    }
    emit(1); SB();
    u32x2 ox[2];
#pragma unroll
    for (int t = 1; t >= 0; --t) {
#pragma unroll
      for (int h = 2; h < 4; ++h) { ex[t] = (__builtin_bit_cast(u32x4, e[t][h]) & ac.gm[h]) | (ex[t] & ~ac.gm[h]); rx[t] = (ubits(rl[t][h]) & ac.gm[h]) | (rx[t] & ~ac.gm[h]); }
      ox[t] = pk4(__builtin_bit_cast(f32x4, ex[t]) * fbits(rx[t]));
    }
    put(4, u32x4{ox[0][0], ox[0][1], ox[1][0], ox[1][1]});
    {
      const u32x4 c4 = {(unsigned)__builtin_amdgcn_mov_dpp((int)cs, 0x00, 0xf, 0xf, true), (unsigned)__builtin_amdgcn_mov_dpp((int)cs, 0x55, 0xf, 0xf, true),
                        (unsigned)__builtin_amdgcn_mov_dpp((int)cs, 0xaa, 0xf, 0xf, true), (unsigned)__builtin_amdgcn_mov_dpp((int)cs, 0xff, 0xf, 0xf, true)};
      if ((lane & 3) == 0) __builtin_amdgcn_raw_buffer_store_b128(c4, orsrc, lane * 4, obase + 5120, SYS);
    }
    STAMP(3);
    setup4();
    bar();
    pre_read();
    STAMP(10);
    asm volatile("s_waitcnt vmcnt(0)" ::: "memory");
    STAMP(11);
    bar_l();
    project(IC(0));
  }
  const u64 tag = mktag(epoch);
  if (__builtin_expect(__builtin_amdgcn_ballot_w64(((unsigned)(gv[0] >> 32) ^ (unsigned)tag) | ((unsigned)(gv[1] >> 32) ^ (unsigned)tag) | ((unsigned)(gv[2] >> 32) ^ (unsigned)tag)) != 0, 0)) {
#pragma unroll
    for (int e = 0; e < 3; ++e)
#pragma unroll 1
      for (int spin = 0; spin < SPIN; ++spin) { gv[e] = __hip_atomic_load((const GAS u64*)(ws + go[e]), RLX); if (__builtin_amdgcn_ballot_w64((unsigned)(gv[e] >> 32) != (unsigned)tag) == 0) break; }
  }
  char* const out = (char*)ptr_of(ka, 6);
  auto finish = [&](auto E) AI {
    constexpr int e = E.value;
    LAS char* pr = prd[e];
    const float gt = fbits((unsigned)gv[e]), gn = rbf(1.f - gt);
    const f32x4 lo = (*(LAS f32x4*)(pr) + *(LAS f32x4*)(pr + 10240)) * gt + (*(LAS f32x4*)(pr + 20480) + *(LAS f32x4*)(pr + 30720)) * gn;
    const f32x4 hi = (*(LAS f32x4*)(pr + 5120) + *(LAS f32x4*)(pr + 15360)) * gt + (*(LAS f32x4*)(pr + 25600) + *(LAS f32x4*)(pr + 35840)) * gn;
    const u32x2 l2 = pk4(lo), h2 = pk4(hi);
    __builtin_amdgcn_raw_buffer_store_b128(u32x4{l2[0], l2[1], h2[0], h2[1]}, rsrc(out, 2048 * 1280), oo[e], 0, SYS);
  };
  if (!th) {
    __builtin_amdgcn_s_setprio(0);
    bar_l();
    finish(IC(0));
    if (wid == 0) finish(IC(1));
  }
  bar_l();
  if (wid < 5) finish(IC(2));
  if (hg == 0 && tid == 0) __hip_atomic_store((GAS u64*)(ws + WS_E + tg * 128), tag, __ATOMIC_RELAXED, __HIP_MEMORY_SCOPE_SYSTEM);
  STAMP(8);
}

extern "C" void kernel_launch(void* const* d_in, const int* in_sizes, int n_in, void* d_out, int out_size, void* d_ws, size_t ws_size, hipStream_t stream) {
  (void)in_sizes; (void)n_in; (void)out_size; (void)ws_size;
  (void)hipFuncSetAttribute((const void*)fused_kernel, hipFuncAttributeMaxDynamicSharedMemorySize, LDS_BYTES);
  fused_kernel<<<256, 512, LDS_BYTES, stream>>>((const char*)d_in[0], (const char*)d_in[1], (const char*)d_in[4], (const char*)d_in[6], (const char*)d_in[2],
                                                (const char*)d_in[5], (char*)d_out, (char*)d_ws);
}
```

```cpp
#define TOUCH 1
#include <hip/hip_runtime.h>
#include <hip/hip_bf16.h>
#include <utility>

typedef __bf16 bf16x8 __attribute__((ext_vector_type(8)));
typedef __bf16 bf16x4 __attribute__((ext_vector_type(4)));
typedef __bf16 bf16x2 __attribute__((ext_vector_type(2)));
typedef float f32x4 __attribute__((ext_vector_type(4)));
typedef float f32x2 __attribute__((ext_vector_type(2)));
typedef unsigned u32x6 __attribute__((ext_vector_type(6)));
typedef unsigned u32x4 __attribute__((ext_vector_type(4)));
typedef unsigned u32x2 __attribute__((ext_vector_type(2)));
typedef unsigned long long u64;
#define LAS __attribute__((address_space(3)))
#define GAS __attribute__((address_space(1)))
#define DI __device__ __forceinline__
#define AI __attribute__((always_inline))
#define SB() __builtin_amdgcn_sched_barrier(0)
#define PIN(x) asm volatile("" : "+v"(x))
#define RLX __ATOMIC_RELAXED, __HIP_MEMORY_SCOPE_AGENT

constexpr int STG = 49152;
constexpr int XOFF = 30720;
constexpr int EXCH = 143360;
constexpr int GWL = EXCH + 3 * 5120;
constexpr int LDS_BYTES = 163840;
constexpr int WS_E = 0;
constexpr int WS_GATE = 4096;
constexpr int WS_O = 32768;
constexpr int REC = 5376;
constexpr int TGS = 64 * REC + 4096;
constexpr int O_BYTES = 16 * TGS;
constexpr int SPIN = 1 << 16;
constexpr int SYS = 17, DEV = 16;
constexpr unsigned CODE_BYTES = 28672;
#define STAMP(i)

DI void bar() { asm volatile("s_barrier" ::: "memory"); }
DI void bar_l() { __builtin_amdgcn_s_waitcnt(0xc07f); asm volatile("s_barrier" ::: "memory"); }
template <int N> DI void bar_v() { asm volatile("s_waitcnt vmcnt(%0)\n\ts_barrier" :: "i"(N) : "memory"); }
DI bf16x8 cat(bf16x4 a, bf16x4 b) { return __builtin_shufflevector(a, b, 0, 1, 2, 3, 4, 5, 6, 7); }
DI bf16x8 cat(u32x2 a, u32x2 b) { u32x4 r = {a[0], a[1], b[0], b[1]}; return __builtin_bit_cast(bf16x8, r); }
DI u32x6 cat3(u32x2 a, u32x2 b, u32x2 c) { u32x6 r = {a[0], a[1], b[0], b[1], c[0], c[1]}; return r; }
DI bf16x8 lo4(u32x6 t) { return __builtin_bit_cast(bf16x8, __builtin_shufflevector(t, t, 0, 1, 2, 3)); }
DI bf16x8 hi4(u32x6 t) { return __builtin_bit_cast(bf16x8, __builtin_shufflevector(t, t, 2, 3, 4, 5)); }
DI bf16x4 trd(LAS char* p) { return __builtin_amdgcn_ds_read_tr16_b64_v4bf16((LAS bf16x4*)p); }
DI unsigned pk2(float a, float b) { f32x2 v = {a, b}; return __builtin_bit_cast(unsigned, __builtin_convertvector(v, bf16x2)); }
DI u32x2 pk4(f32x4 v) { u32x2 r = {pk2(v[0], v[1]), pk2(v[2], v[3])}; return r; }
DI f32x4 mfma(bf16x8 a, bf16x8 b, f32x4 c) { return __builtin_amdgcn_mfma_f32_16x16x32_bf16(a, b, c, 0, 0, 0); }
DI float rbf(float v) { return __builtin_bit_cast(float, pk2(v, v) << 16); }
DI u64 mktag(u64 e) { return e * 6364136223846793005ull + 1442695040888963407ull; }
DI unsigned sum4(u32x4 v) { return v[0] + v[1] + v[2] + v[3]; }
DI float fmx(float a, float b) { return __builtin_elementwise_maximum(a, b); }
DI float fbits(unsigned u) { return __builtin_bit_cast(float, u); }
DI unsigned ubits(float f) { return __builtin_bit_cast(unsigned, f); }
template <class F, int... I> DI void sfor_(F&& f, std::integer_sequence<int, I...>) { (f(std::integral_constant<int, I>{}), ...); }
template <int N, class F> DI void sfor(F&& f) { sfor_(f, std::make_integer_sequence<int, N>{}); }
#define IC(v) std::integral_constant<int, (v)>{}

constexpr int CARRY = 4;
constexpr int PRE = 1;
constexpr int EGAP = 5;
constexpr int GATE_F = 4;
constexpr bool has_halo(int W, int f) { return f >= 5 && ((f - 5) & 3) == W; }
constexpr int grp_n(int W, int n) { return (has_halo(W, n % 15) || (W == 2 && n % 15 == GATE_F)) ? 3 : 2; }
constexpr int m_grp(int W, int m) { int n = 30 - CARRY; while (m >= grp_n(W, n % 30)) { m -= grp_n(W, n % 30); ++n; } return n - (30 - CARRY); }
constexpr int m_idx(int W, int m) { int n = 30 - CARRY; while (m >= grp_n(W, n % 30)) { m -= grp_n(W, n % 30); ++n; } return m; }
constexpr int n_mfma(int W) { int t = 0; for (int n = 0; n < 30; ++n) t += grp_n(W, n); return t; }
constexpr int n_read(int W) { return W == 2 ? 68 : 66; }
constexpr int rd_code(int W, int j) {
  constexpr int pos[2][10] = {{0, 3, 10, -1, -1, 29, 32, 41, -1, -1}, {0, 3, 8, -1, 11, 30, 33, 40, -1, 43}};
  int k = 0;
  for (int i = 0; i < 10; ++i) { const int p = pos[W == 2][i]; if (p < 0) continue; if (j == p) return 60 + i; if (j > p) ++k; }
  return j - k;
}

template <int W>
DI void gemm1(LAS char* lds, int lane, int hg, f32x4 (&acc)[2][15], float& logit) {
  const int g = lane >> 4, i16 = lane & 15, q = i16 >> 2, p = lane & 3;
  const unsigned rowb = (16 * (g >> 1) + 4 * (g & 1) + q) * 480;
  const unsigned aM = rowb + 8 * p, aX = rowb + 40 * p + 32;
  const unsigned aA = XOFF + i16 * 128 + ((g ^ (i16 >> 1)) * 16);
  const int rh = i16 < 8 ? 16 + 8 * hg + i16 : i16;
  const unsigned aH = XOFF + rh * 128 + ((g ^ ((rh >> 1) & 7)) * 16);
  f32x4 hal[3];
#pragma unroll
  for (int t = 0; t < 2; ++t)
#pragma unroll
    for (int n = 0; n < 15; ++n) acc[t][n] = f32x4{0, 0, 0, 0};
#pragma unroll
  for (int n = 0; n < 3; ++n) hal[n] = f32x4{0, 0, 0, 0};
  bf16x8 cw[CARRY], cx[3];
  const bf16x8 z8 = __builtin_bit_cast(bf16x8, u32x4{0, 0, 0, 0});
#pragma unroll
  for (int n = 0; n < CARRY; ++n) cw[n] = z8;
#pragma unroll
  for (int n = 0; n < 3; ++n) cx[n] = z8;
  auto one = [&](int f, int i, bf16x8 wf, bf16x8 xf) AI {
    f32x4& d = i < 2 ? acc[i][f] : hal[(f - 5) >> 2];
    d = f < 10 ? mfma(wf, xf, d) : mfma(xf, wf, d);
  };
  constexpr int NM = n_mfma(W), NR = n_read(W), NP = W == 2 ? 7 : 6;
  unsigned buf = 0;
  LAS char* pp[7] = {lds + aM, lds + aX, lds + aA, lds + (aA ^ 64), lds + aH, lds + (aH ^ 64), lds + (GWL + g * 16)};
  int it = 10, dl = STG;
#pragma unroll 1
  do {
    bf16x4 wh[60]; bf16x8 xf[10];
    auto rd = [&](auto J) AI {
      constexpr int c = rd_code(W, J.value);
      if constexpr (c < 60) {
        constexpr int n = c >> 1, ks = n / 15, f = n % 15, sec = f / 5, jj = f % 5;
        wh[c] = trd((jj < 4 ? pp[0] + 40 * jj : pp[1]) + (ks * 32 + (c & 1) * 8) * 480 + sec * 160);
      } else {
        constexpr int ks = (c - 60) / 5, t = (c - 60) % 5;
        xf[c - 60] = *(LAS bf16x8*)(t < 2 ? pp[2 + ks] + (2 * W + 1 + t) * 2048 : t == 2 ? pp[4 + ks] : pp[6] + ks * 64);
      }
    };
    sfor<NM>([&](auto M) AI {
      constexpr int m = M.value, gi = m_grp(W, m), i0 = m_idx(W, m), i = (i0 < 2 && (gi & 1)) ? 1 - i0 : i0;
      if constexpr (m == PRE) { bar_l(); SB(); }
      if constexpr (gi < CARRY) one(15 - CARRY + gi, i, cw[gi], cx[i]);
      else {
        constexpr int n = gi - CARRY, ks = n / 15;
        if constexpr (W == 2 && n % 15 == GATE_F && i == 2) hal[2] = mfma(xf[ks * 5 + 4], xf[ks * 5 + 2], hal[2]);
        else one(n % 15, i, cat(wh[2 * n], wh[2 * n + 1]), xf[ks * 5 + i]);
      }
      SB();
      constexpr int D2 = NR - (NM - EGAP), j = m - PRE;
      if constexpr (j >= 0 && j < D2) { rd(IC(2 * j)); rd(IC(2 * j + 1)); }
      else if constexpr (j >= 0 && D2 + j < NR) rd(IC(D2 + j));
      else if constexpr (j >= 0) {
        constexpr int u = m - (NM - (EGAP - PRE)), NG = EGAP - PRE, PER = (NP + NG - 1) / NG;
        sfor<PER>([&](auto K) AI { constexpr int k = u * PER + K.value; if constexpr (k < NP) { pp[k] += (k == 6 ? 128 : dl); PIN(pp[k]); } });
      }
      if constexpr (m == 21) { dl = (buf == 2 * STG) ? -2 * STG : STG; asm volatile("" : "+s"(dl)); }
      if constexpr (m == 23) { buf += dl; --it; asm volatile("" : "+s"(buf), "+s"(it)); }
      SB();
    });
#pragma unroll
    for (int n = 0; n < CARRY; ++n) cw[n] = cat(wh[2 * (30 - CARRY + n)], wh[2 * (30 - CARRY + n) + 1]);
#pragma unroll
    for (int n = 0; n < 3; ++n) cx[n] = xf[5 + n];
  } while (it);
#pragma unroll
  for (int gi = 0; gi < CARRY; ++gi)
#pragma unroll
    for (int i = 0; i < 3; ++i)
      if (i < grp_n(W, 30 - CARRY + gi)) one(15 - CARRY + gi, i, cw[gi], cx[i]);
#pragma unroll
  for (int f = 5; f < 15; ++f)
    if (has_halo(W, f)) *(LAS u32x2*)(lds + EXCH + (f - 5) * 512 + lane * 8) = pk4(hal[(f - 5) >> 2]);
  if (W == 2) logit = hal[2][0];
}

struct AttnConst { f32x4 biasC, biasP, biasP0; unsigned selC[2], selP[2], gm[4]; };
DI bf16x8 q_operand(int h, u32x2 qh, u32x2 qx, unsigned gm) { const u32x2 qm = {qx[0] & gm, qx[1] & gm}; return (h & 1) ? cat(qm, qh) : cat(qh, qm); }
DI bf16x8 k_operand(int h, const u32x6 (&k)[2]) { return (h & 1) ? hi4(k[h >> 1]) : lo4(k[h >> 1]); }
#define PW "s_waitcnt vmcnt(3)\n\t"
#define DMA(v, srd, so, mo, si, mi) "s_add_u32 m0, " mo ", " #mi "\n\ts_add_u32 %0, " so ", " #si "\n\tbuffer_load_dwordx4 " v ", " srd ", %0 offen lds\n\t"

typedef unsigned u32x16 __attribute__((ext_vector_type(16)));
DI const char* ptr_of(u32x16 k, int i) { return (const char*)(((u64)k[2 * i + 1] << 32) | k[2 * i]); }
DI __amdgpu_buffer_rsrc_t rsrc(const char* p, int bytes) { return __builtin_amdgcn_make_buffer_rsrc((void*)p, 0, bytes, 0x00020000); }

extern "C" __global__ __launch_bounds__(512, 2) void fused_kernel(const char*, const char*, const char*, const char*, const char*, const char*, char*, char*) {
  extern __shared__ __attribute__((aligned(16))) char smem[];
  u32x16 ka;
  asm volatile("s_load_dwordx16 %0, %1, 0x0" : "=&s"(ka) : "s"(__builtin_amdgcn_kernarg_segment_ptr()));
  LAS char* lds = (LAS char*)smem;
  const unsigned ldsb = (unsigned)(size_t)lds;
  const int tid = threadIdx.x, lane = tid & 63;
  const int wid = __builtin_amdgcn_readfirstlane(tid >> 6);
  const int b = blockIdx.x, xcd = b & 7, r = b >> 3;
  const int tg = (xcd & 1) * 8 + (r & 7), hg = (xcd >> 1) * 4 + (r >> 3);
  const int par = tg >> 3, ch = tg & 7;
  const int kq = wid & 3, th = wid >> 2;
#define CNT(i) (void)0
  STAMP(0);
  if (wid == 2) {
    const char* pc; unsigned back;
    asm volatile("s_getpc_b64 %0\nfk_here:\n\ts_mov_b32 %1, fk_here-fused_kernel" : "=s"(pc), "=s"(back));
    pc -= back;
    const unsigned po = (r * 8 + (lane & 7)) * 128;
    if (lane < 8 && po < CODE_BYTES) asm volatile("s_mov_b32 m0, %2\n\ts_nop 0\n\tglobal_load_lds_dword %0, %1" :: "v"(po), "s"(pc), "s"(ldsb + XOFF) : "memory");
  }
  const int stg0 = (((5 * ((ch + hg) & 3) + 1) >> 1) + par + ((hg >> 2) & 1)) % 10;
  const int l30 = lane >= 30 ? lane - 30 : lane, sec = (l30 >= 10) + (l30 >= 20);
  unsigned vW = (lane >= 30 ? 3840 : 0) + sec * (1280 - 160) + l30 * 16;
  unsigned vX0 = (lane >> 3) * 2560 + (((lane & 7) ^ (lane >> 4)) * 16), vX1 = vX0 ^ 64;
  unsigned vH = (lane >> 3) * 2560 + (((lane & 7) ^ (4 + kq)) * 16);
  unsigned oW = kq * 16 * 3840 + (hg & 7) * 160;
  unsigned oX = (2 * (128 * ch + 32 * kq) + par) * 1280;
  unsigned oH = (2 * (128 * ch + 2 * kq - (ch ? 8 : 0)) + par) * 1280;
  unsigned sW0 = oW + stg0 * (64 * 3840), sX0 = oX + stg0 * 128, sH0 = oH + stg0 * 128;
  unsigned dW = ldsb + kq * 7680, dX = ldsb + XOFF + (16 + 32 * kq) * 128, dH = ldsb + XOFF + (8 + 2 * kq) * 128;
  asm volatile("s_waitcnt lgkmcnt(0)" : "+s"(ka), "+v"(vW), "+v"(vX0), "+v"(vX1), "+v"(vH), "+s"(sW0), "+s"(sX0), "+s"(sH0), "+s"(dW), "+s"(dX), "+s"(dH));
  const char* const x = ptr_of(ka, 0);
  char* const ws = (char*)ptr_of(ka, 7);
  u64 epoch, epoch_in;
  asm volatile("s_load_dwordx2 %0, %1, 0x0" : "=&s"(epoch_in) : "s"(ws + WS_E + tg * 128) : "memory");

  const auto wsrd = rsrc(hg >= 8 ? ptr_of(ka, 2) : ptr_of(ka, 1), 640 * 3840), xsrd = rsrc(x, 2048 * 1280);
  auto partA = [&](unsigned sw_, unsigned dw) AI {
    unsigned t;
    asm volatile(
      "s_mov_b32 exec_hi, 0x0fffffff\n\t"
      DMA("%1", "%2", "%3", "%4", 0, 0) DMA("%1", "%2", "%3", "%4", 7680, 960) DMA("%1", "%2", "%3", "%4", 30720, 1920)
      DMA("%1", "%2", "%3", "%4", 38400, 2880) DMA("%1", "%2", "%3", "%4", 15360, 3840) DMA("%1", "%2", "%3", "%4", 23040, 4800)
      "s_mov_b32 exec_hi, -1"
      : "=&s"(t) : "v"(vW), "s"(wsrd), "s"(sw_), "s"(dw) : "scc", "memory");
  };
  auto partB = [&](unsigned sw_, unsigned sx, unsigned sh, unsigned dw, unsigned dx, unsigned dh) AI {
    unsigned t;
    asm volatile(
      DMA("%2", "%6", "%8", "%11", 0, 0) DMA("%3", "%6", "%8", "%11", 20480, 1024) DMA("%2", "%6", "%8", "%11", 40960, 2048) DMA("%3", "%6", "%8", "%11", 61440, 3072)
      "s_mov_b32 exec_hi, 0x0fffffff\n\t"
      DMA("%1", "%5", "%7", "%10", 46080, 5760) DMA("%1", "%5", "%7", "%10", 53760, 6720)
      "s_mov_b64 exec, 0xffff\n\t"
      DMA("%4", "%6", "%9", "%12", 0, 0)
      "s_mov_b64 exec, -1"
      : "=&s"(t)
      : "v"(vW), "v"(vX0), "v"(vX1), "v"(vH), "s"(wsrd), "s"(xsrd), "s"(sw_), "s"(sx), "s"(sh), "s"(dw), "s"(dx), "s"(dh)
      : "scc", "memory");
  };
  if (wid >= 4) {
    STAMP(14);
    partB(sW0, sX0, sH0, dW, dX, dH); partA(sW0, dW);
    if (kq < 2) {
      const int c = kq * 64 + lane, sg = stg0 + (c >> 3);
      unsigned t;
      asm volatile(DMA("%1", "%2", "0", "%3", 0, 0) : "=&s"(t)
                   : "v"(c < 80 ? (sg >= 10 ? sg - 10 : sg) * 128 + (c & 7) * 16 : 2048), "s"(rsrc(ptr_of(ka, 3), 1280)), "s"(ldsb + GWL + kq * 1024) : "scc", "memory");
    }
    partA(stg0 == 9 ? oW : sW0 + 64 * 3840, dW + STG);
    STAMP(9);
  }
  SB();
  const int g = lane >> 4, i16 = lane & 15;
  const int hf = (hg >> 2) & 1, ct = (hg & 3) + 4 * (hg >> 3);
  unsigned so[4][3];
  LAS char *pw, *pw3;
  auto setup3 = [&]() AI {
    int tq = tg; asm volatile("" : "+s"(tq));
#pragma unroll
    for (int i = 0; i < 4; ++i) {
      const unsigned ob = tq * TGS + ((4 * kq + ((tq + i) & 3)) * 4 + 2 * hf + th) * REC;
      so[i][0] = ob + (tq & 4) * 256; so[i][1] = ob + ((tq & 4) ^ 4) * 256; so[i][2] = ob + 4096;
      asm volatile("" : "+s"(so[i][0]), "+s"(so[i][1]), "+s"(so[i][2]));
    }
  };
  unsigned oo[3], go[3]; LAS char *pwr, *prd[3];
  auto setup4 = [&]() AI {
    int td = tid; PIN(td);
#pragma unroll
    for (int e = 0; e < 3; ++e) {
      const int c = e == 1 ? 256 + (td & 63) : td < 320 ? td : 0, tk = c / 10, tok = tk + (e == 2 ? 32 : 0), j = c - tk * 10;
      oo[e] = (2 * (128 * ch + 64 * hf + tok) + par) * 1280 + ct * 160 + j * 16; PIN(oo[e]);
      go[e] = WS_GATE + (tg * 128 + 64 * hf + tok) * 8; PIN(go[e]);
      prd[e] = lds + ((e == 2 ? 0 : 122880) + (c ^ ((tk >> 2) & 1)) * 16); PIN(prd[e]);
    }
    pwr = lds + ((th ? 0 : 122880) + kq * 10240 + ((td >> 4) & 1) * 5120 + (td & 15) * 160 + (((td >> 5) ^ (td >> 2)) & 1) * 16);
    PIN(pwr);
  };
  bf16x8 pre[4][5];
  auto pre_read = [&]() AI {
    int ln = lane; PIN(ln);
    pw = lds + ((ln >> 2) * 160 + 8 * (ln & 3) + kq * 5120); pw3 = pw + 3 * 20480;
    PIN(pw); PIN(pw3);
#pragma unroll
    for (int ps = 0; ps < 4; ++ps)
#pragma unroll
      for (int c5 = 0; c5 < 5; ++c5) pre[ps][c5] = cat(trd(pw3 + ps * 20480 + c5 * 32), trd(pw3 + ps * 20480 + c5 * 32 + 2560));
  };
  u32x4 late[3][5];

  u64 gv[3];
  auto project = [&](auto THC) AI {
    constexpr bool TH = THC.value;
    STAMP(4);
    const u64 tag = mktag(epoch);
    const auto orsrc = rsrc(ws + WS_O, O_BYTES);
    struct Rec { u32x4 m[2][2], x; unsigned cs; };
    Rec rec[4];
    const unsigned l16 = lane * 16, l4 = lane * 4;
    auto ldrec = [&](int i, int j) AI {
      if (j < 4) rec[i].m[j >> 1][j & 1] = __builtin_amdgcn_raw_buffer_load_b128(orsrc, l16 + (j & 1) * 2048, so[i][j >> 1], DEV);
      else if (j == 4) rec[i].x = __builtin_amdgcn_raw_buffer_load_b128(orsrc, l16, so[i][2], DEV);
      else rec[i].cs = __builtin_amdgcn_raw_buffer_load_b32(orsrc, l4 + 1024, so[i][2], DEV);
    };
    auto reload = [&](int i) AI {
      Rec& q = rec[i];
      asm volatile("buffer_load_dwordx4 %0, %6, %8, %9 offen sc1\n\tbuffer_load_dwordx4 %1, %6, %8, %9 offen offset:2048 sc1\n\t"
                   "buffer_load_dwordx4 %2, %6, %8, %10 offen sc1\n\tbuffer_load_dwordx4 %3, %6, %8, %10 offen offset:2048 sc1\n\t"
                   "buffer_load_dwordx4 %4, %6, %8, %11 offen sc1\n\tbuffer_load_dword %5, %7, %8, %11 offen offset:1024 sc1\n\ts_waitcnt vmcnt(0)"
                   : "+v"(q.m[0][0]), "+v"(q.m[0][1]), "+v"(q.m[1][0]), "+v"(q.m[1][1]), "+v"(q.x), "+v"(q.cs)
                   : "v"(l16), "v"(l4), "s"(orsrc), "s"(so[i][0]), "s"(so[i][1]), "s"(so[i][2]) : "memory");
    };
    auto word = [&](int i, int k) AI { return k < 16 ? rec[i].m[k >> 3][(k >> 2) & 1][k & 3] : rec[i].x[k & 3]; };
    auto stale = [&](int i) AI {
      unsigned c = (unsigned)tag;
#pragma unroll
      for (int k = 0; k < 20; ++k) c += word(i, k);
      return __builtin_amdgcn_ballot_w64(c != rec[i].cs);
    };
    auto refetch = [&](int i, int n) AI {
#pragma unroll 1
      for (int spin = 0; spin < SPIN; ++spin) { CNT(i); reload(i); if (!stale(i)) break; }
#pragma unroll
      for (int k = 1; k < n; ++k) reload(i + k);
    };
    __builtin_amdgcn_s_setprio(TH ? 1 : 2);
    if constexpr (TH) {
      LAS char* lp = lds + 3 * 20480 + kq * 5120 + lane * 16;
#pragma unroll
      for (int ps = 0; ps < 3; ++ps)
#pragma unroll
        for (int i = 0; i < 5; ++i) *(LAS u32x4*)(lp + ((ps + 2) % 3) * 20480 + i * 1024) = late[(ps + 2) % 3][i];
      SB(); setup3(); setup4();
      bar_l();
    }
    SB();
    sfor<12>([&](auto J) AI { ldrec(J.value / 6, J.value % 6); SB(); });
    STAMP(6);
    SB();
    bf16x4 wq[10][10];
    auto rdq = [&](auto P, auto I) AI {
      constexpr int G[10] = {3, 4, 5, 6, 0, 1, 2, 3, 4, 5};
      wq[P.value][I.value] = trd((G[P.value] < 3 ? pw + G[P.value] * 20480 : pw3 + (G[P.value] - 3) * 20480) + (I.value >> 1) * 32 + (I.value & 1) * 2560);
    };
    SB();
    if (__builtin_expect(stale(0) != 0, 0)) refetch(0, 2);
    if constexpr (TH) { if (__builtin_expect(stale(1) != 0, 0)) refetch(1, 1); }
    STAMP(5);
    f32x4 a3[2][5];
    unsigned csum = 0; u64 bad = 0; bf16x8 xo[2];
    sfor<100>([&](auto M) AI {
      constexpr int m = M.value;
      constexpr int pos = m < 70 ? m / 10 : 7 + ((m - 70) % 6) / 2, c5 = m < 70 ? (m % 10) >> 1 : (m - 70) / 6, t = m & 1;
      constexpr int pp = pos < 4 ? pos : pos - 1;
      if constexpr ((m == 20 && !TH) || m == 50 || m == 70) { constexpr int i = m == 20 ? 1 : m == 50 ? 2 : 3; if (__builtin_expect(bad != 0, 0)) refetch(i, i == 1 ? 1 : 4 - i); SB(); }
      if constexpr (m == 80 && TH) { bar(); SB(); }
      const bf16x8 ov = (pos == 4 || pos == 9) ? xo[t] : __builtin_bit_cast(bf16x8, rec[pp >> 1].m[pp & 1][t]);
      const bf16x8 wv = pos < 4 ? pre[pos & 3][c5] : cat(wq[pos][2 * c5], wq[pos][2 * c5 + 1]);
      if constexpr (m < 10) asm volatile("v_mfma_f32_16x16x32_bf16 %0, %1, %2, 0" : "=&v"(a3[t][c5]) : "v"(wv), "v"(ov));
      else asm volatile("v_mfma_f32_16x16x32_bf16 %0, %1, %2, %0" : "+v"(a3[t][c5]) : "v"(wv), "v"(ov));
      SB();
      if constexpr (m < 12) ldrec(2 + m / 6, m % 6);
      if constexpr (!TH && m == 12) bar();
      if constexpr (!TH && m > 12 && m < 16) gv[m - 13] = __hip_atomic_load((const GAS u64*)(ws + go[m - 13]), RLX);
      if constexpr (TH && m == 12) { gv[2] = __hip_atomic_load((const GAS u64*)(ws + go[2]), RLX); gv[0] = gv[1] = gv[2]; }
      if constexpr (m >= 20 && m < 50) rdq(IC(m / 10 + 2), IC(m % 10));
      if constexpr (m >= 50 && m < 80) rdq(IC(7 + ((m - 50) % 6) / 2), IC(2 * ((m - 50) / 6) + (m & 1)));
      constexpr int ci = m < 20 ? 1 : m < 50 ? 2 : 3, cb = m < 20 ? 8 : m < 50 ? 36 : 56, k = m - cb;
      if constexpr (m < 70 && (m >= 20 || !TH) && k >= 0 && k < 10) { csum = (k ? csum : (unsigned)tag) + word(ci, 2 * k) + word(ci, 2 * k + 1); PIN(csum); }
      if constexpr (m < 70 && (m >= 20 || !TH) && k == 10) { bad = __builtin_amdgcn_ballot_w64(csum != rec[ci].cs); asm volatile("" : "+s"(bad)); }
      if constexpr (m == 30 || m == 31 || m == 70 || m == 71) {
        constexpr int r0 = m < 70 ? 0 : 2;
        xo[t] = cat(u32x2{rec[r0].x[2 * t], rec[r0].x[2 * t + 1]}, u32x2{rec[r0 + 1].x[2 * t], rec[r0 + 1].x[2 * t + 1]}); PIN(xo[t]);
      }
      if constexpr (m >= 82 && (m - 82) % 6 < 2) *(LAS f32x4*)(pwr + t * 16 * 160 + ((m - 82) / 6) * 32) = a3[t][(m - 82) / 6];
      SB();
    });
    *(LAS f32x4*)(pwr + 3 * 32) = a3[0][3];
    asm volatile("s_nop 1" : "+v"(a3[1][3]));
    *(LAS f32x4*)(pwr + 16 * 160 + 3 * 32) = a3[1][3];
    asm volatile("s_nop 5" : "+v"(a3[0][4]), "+v"(a3[1][4]));
    *(LAS f32x4*)(pwr + 4 * 32) = a3[0][4];
    *(LAS f32x4*)(pwr + 16 * 160 + 4 * 32) = a3[1][4];
    STAMP(7);
  };

  if (__builtin_expect(wid >= 4, 0)) {
    int s1 = stg0 == 9 ? 0 : stg0 + 1, s2 = stg0 >= 8 ? stg0 - 8 : stg0 + 2;
    const auto wosrd = rsrc(kq >> 1 ? ptr_of(ka, 5) : ptr_of(ka, 4), 640 * 1280);
    const int rot = tg & 3, sw = (tg >> 2) & 1;
    unsigned vm[5], ve[5];
#pragma unroll
    for (int i = 0; i < 5; ++i) {
      const int c = i * 64 + lane, rr = c / 10, wi = c - rr * 10;
      vm[i] = (20 * (rr >> 4) + (rr & 15)) * 1280 + wi * 16; PIN(vm[i]);
      ve[i] = (20 * ((rr >> 2) & 3) + 16 + (rr & 3)) * 1280 + wi * 16; PIN(ve[i]);
    }
    auto wo_off = [&](int pos, unsigned (&v)[5], unsigned& sq) AI {
      if (pos == 4 || pos == 9) {
        const unsigned ha = (4 * (kq & 1) + ((rot + (pos == 9 ? 2 : 0)) & 3)) * 102400, hb = (4 * (kq & 1) + ((rot + (pos == 9 ? 3 : 1)) & 3)) * 102400;
#pragma unroll
        for (int i = 0; i < 5; ++i) v[i] = ve[i] + ((i * 64 + lane >= 160) ? hb : ha);
        sq = ct * 160;
      } else {
        const int pp = pos < 4 ? pos : pos - 1;
#pragma unroll
        for (int i = 0; i < 5; ++i) v[i] = vm[i];
        sq = ct * 160 + (4 * (kq & 1) + ((rot + (pp >> 1)) & 3)) * 102400 + ((pp & 1) ^ sw) * 51200;
      }
    };
    auto wo_pos = [&](int pos, int grp, bool paced) AI {
      unsigned v[5], sq, t;
      wo_off(pos, v, sq);
#define WO5(P) asm volatile(P DMA("%1", "%6", "%7", "%8", 0, 0) P DMA("%2", "%6", "%7", "%8", 0, 1024) P DMA("%3", "%6", "%7", "%8", 0, 2048) P DMA("%4", "%6", "%7", "%8", 0, 3072) P DMA("%5", "%6", "%7", "%8", 0, 4096) \
                   : "=&s"(t) : "v"(v[0]), "v"(v[1]), "v"(v[2]), "v"(v[3]), "v"(v[4]), "s"(wosrd), "s"(sq), "s"(ldsb + grp * 20480 + kq * 5120) : "scc", "memory")
      if (paced) WO5(PW); else WO5("");
    };
    auto late_load = [&](int ps, bool paced) AI {
      unsigned v[5], sq;
      wo_off(7 + ps, v, sq);
#pragma unroll
      for (int i = 0; i < 5; ++i) { if (paced) asm volatile(PW ::: "memory"); late[ps][i] = __builtin_amdgcn_raw_buffer_load_b128(wosrd, v[i], sq, 0); }
    };
    unsigned b1 = STG, b2 = 2 * STG;
    unsigned q0, q1, q2, q3, q4, q5, q6, q7;
    auto prep = [&]() AI {
      q0 = oW + s1 * (64 * 3840); q1 = oX + s1 * 128; q2 = oH + s1 * 128; q3 = dW + b1; q4 = dX + b1; q5 = dH + b1; q6 = oW + s2 * (64 * 3840); q7 = dW + b2;
      asm volatile("" : "+s"(q0), "+s"(q1), "+s"(q2), "+s"(q3), "+s"(q4), "+s"(q5), "+s"(q6), "+s"(q7));
      s1 = s2; s2 = (s2 == 9) ? 0 : s2 + 1;
      b1 = b2; b2 = (b2 == 2 * STG) ? 0 : b2 + STG;
    };
    prep();
    asm volatile("s_waitcnt vmcnt(6) lgkmcnt(0)\n\ts_mov_b64 %0, %1" : "=s"(epoch) : "s"(epoch_in) : "memory");
#pragma unroll 1
    for (int it = 0; it < 8; ++it) {
      SB(); bar(); SB();
      partB(q0, q1, q2, q3, q4, q5); partA(q6, q7);
      SB();
      prep();
      asm volatile("s_waitcnt vmcnt(6)" ::: "memory");
    }
    bar();
    STAMP(19);
    partB(q0, q1, q2, q3, q4, q5);
    wo_pos(0, 3, false);
    bar_v<5>();
    STAMP(13);
    wo_pos(1, 4, false); wo_pos(2, 5, false); wo_pos(3, 6, false);
    late_load(2, false);
    STAMP(1);
    bar();
    STAMP(2);
    wo_pos(4, 0, false); wo_pos(5, 1, true); wo_pos(6, 2, true);
    late_load(0, true);
    STAMP(3);
    bar();
    late_load(1, false);
    pre_read();
    __builtin_amdgcn_s_waitcnt(0xc07f);
    bar_v<10>();
    project(IC(1));
  } else {
    const float NINF = -__builtin_inff();
    AttnConst ac;
    const bool first = (ch == 0) && (wid == 0);
#pragma unroll
    for (int j = 0; j < 4; ++j) {
      const int c = 4 * g + j;
      ac.biasC[j] = (c <= i16 && c >= i16 - 7) ? 0.f : NINF;
      ac.biasP[j] = (c >= i16 + 9) ? 0.f : NINF;
      ac.biasP0[j] = first ? NINF : ac.biasP[j];
      ac.gm[j] = (g == j) ? 0xffffffffu : 0u; PIN(ac.gm[j]);
    }
#pragma unroll
    for (int j = 0; j < 2; ++j) {
      const bool c0 = 4 * g + 2 * j <= i16, c1 = 4 * g + 2 * j + 1 <= i16;
      ac.selC[j] = (c0 ? 0x0302u : 0x0c0cu) | (c1 ? 0x07060000u : 0x0c0c0000u); PIN(ac.selC[j]);
      ac.selP[j] = (c0 ? 0x0c0cu : 0x0302u) | (c1 ? 0x0c0c0000u : 0x07060000u); PIN(ac.selP[j]);
    }
    PIN(ac.biasC); PIN(ac.biasP); PIN(ac.biasP0);
    setup3();
    if (wid == 1) {
      const char* tp = lane < 2 ? ws + (WS_O - 128 + (tg + lane) * TGS) : lane == 2 ? ws + 2048 : ptr_of(ka, 6) + ((2 * (128 * ch + 64 * hf) + par) * 1280 + (lane & 1) * 126 * 1280);
      if (lane < (TOUCH == 1 ? 3 : 5)) asm volatile("s_mov_b32 m0, %1\n\ts_nop 0\n\tglobal_load_lds_dword %0, off" :: "v"(tp), "s"(ldsb + XOFF + 256) : "memory");
    }
    f32x4 acc[2][15]; float logit = 0.f;
    asm volatile("s_waitcnt lgkmcnt(0)\n\ts_mov_b64 %0, %1" : "=s"(epoch) : "s"(epoch_in) : "memory");
    STAMP(9);
    if (wid == 0) gemm1<0>(lds, lane, hg, acc, logit);
    else if (wid == 1) gemm1<1>(lds, lane, hg, acc, logit);
    else if (wid == 2) gemm1<2>(lds, lane, hg, acc, logit);
    else gemm1<3>(lds, lane, hg, acc, logit);
    asm volatile("s_waitcnt vmcnt(0)" ::: "memory");
    STAMP(1);
    const auto orsrc = rsrc(ws + WS_O, O_BYTES);
    u32x6 K[3][2], V[5];
    auto set2 = [&](u32x6& t, int i, u32x2 v) AI { t[i] = v[0]; t[i + 1] = v[1]; };
    K[2][0] = cat3(pk4(acc[1][5]), pk4(acc[1][9]), pk4(acc[1][6])); PIN(K[2][0]);
    K[2][1] = cat3(pk4(acc[1][7]), u32x2{K[2][0][2], K[2][0][3]}, pk4(acc[1][8])); PIN(K[2][1]);
#pragma unroll
    for (int f = 0; f < 5; ++f) { asm volatile("" : "=v"(V[f])); set2(V[f], 4, pk4(acc[1][10 + f])); PIN(V[f]); }
    if (wid < 3) {
      LAS char* e = lds + EXCH + (wid + 1) * 5120 + lane * 8;
      *(LAS u32x2*)(e) = u32x2{K[2][0][0], K[2][0][1]}; *(LAS u32x2*)(e + 512) = u32x2{K[2][0][4], K[2][0][5]};
      *(LAS u32x2*)(e + 1024) = u32x2{K[2][1][0], K[2][1][1]}; *(LAS u32x2*)(e + 1536) = u32x2{K[2][1][4], K[2][1][5]};
      *(LAS u32x2*)(e + 2048) = u32x2{K[2][0][2], K[2][0][3]};
#pragma unroll
      for (int f = 0; f < 5; ++f) *(LAS u32x2*)(e + (5 + f) * 512) = u32x2{V[f][4], V[f][5]};
    }
    SB();
    const u32x2 qx[2] = {pk4(acc[0][4]), pk4(acc[1][4])};
    bf16x8 qb[2][4]; f32x4 sc[2][4], sp[2][4];
    auto scores = [&](int p) AI {
      K[1][p] = cat3(pk4(acc[0][5 + 2 * p]), p ? u32x2{K[1][0][2], K[1][0][3]} : pk4(acc[0][9]), pk4(acc[0][6 + 2 * p])); PIN(K[1][p]);
#pragma unroll
      for (int t = 1; t >= 0; --t)
#pragma unroll
        for (int h = 2 * p; h < 2 * p + 2; ++h) {
          qb[t][h] = q_operand(h, pk4(acc[t][h]), qx[t], ac.gm[h]);
          sc[t][h] = mfma(k_operand(h, K[t + 1]), qb[t][h], ac.biasC);
          if (t) sp[t][h] = mfma(k_operand(h, K[t]), qb[t][h], ac.biasP);
        }
    };
    scores(0);
    SB();
    bar_l();
    STAMP(2);
    SB();
    {
      LAS char* e = lds + EXCH + wid * 5120 + lane * 8;
      K[0][0] = cat3(*(volatile LAS u32x2*)(e), *(volatile LAS u32x2*)(e + 2048), *(volatile LAS u32x2*)(e + 512));
      set2(V[0], 0, *(volatile LAS u32x2*)(e + 5 * 512)); set2(V[1], 0, *(volatile LAS u32x2*)(e + 6 * 512)); set2(V[4], 0, *(volatile LAS u32x2*)(e + 9 * 512));
      K[0][1] = cat3(*(volatile LAS u32x2*)(e + 1024), *(volatile LAS u32x2*)(e + 2048), *(volatile LAS u32x2*)(e + 1536));
      set2(V[2], 0, *(volatile LAS u32x2*)(e + 7 * 512)); set2(V[3], 0, *(volatile LAS u32x2*)(e + 8 * 512));
    }
    const unsigned obase = tg * TGS + (hg * 4 + wid) * REC;
    unsigned cs = (unsigned)mktag(epoch);
    auto put = [&](int k, u32x4 v) AI {
      __builtin_amdgcn_raw_buffer_store_b128(v, orsrc, lane * 16 + (k & 3) * 1024, obase + (k >> 2) * 4096, SYS);
      cs += sum4(v);
    };
    const float CS = 0.22360679774997896f * 1.4426950408889634f;
    const bf16x8 ones = __builtin_bit_cast(bf16x8, u32x4{0x3f803f80u, 0x3f803f80u, 0x3f803f80u, 0x3f803f80u});
    const f32x4 z4 = {0, 0, 0, 0};
    f32x4 s[2][4], l[2][4], o[2][4], e[2][4]; float mc[2][4], rl[2][4]; u32x2 op[2][4];
    auto maxima = [&](int p) AI {
      unsigned ml[4];
#pragma unroll
      for (int h = 2 * p; h < 2 * p + 2; ++h) { if (h == 2 * p) PIN(K[0][p]); sp[0][h] = mfma(k_operand(h, K[0]), qb[0][h], ac.biasP0); }
#pragma unroll
      for (int u = 3; u >= 0; --u) {
        const int t = u >> 1, h = 2 * p + (u & 1);
        s[t][h] = __builtin_elementwise_maximum(sc[t][h], sp[t][h]);
        ml[u] = ubits(fmx(fmx(fmx(s[t][h][0], s[t][h][1]), s[t][h][2]), s[t][h][3]));
      }
      auto r2 = __builtin_amdgcn_permlane16_swap(ml[2], ml[3], false, false);
      auto r1 = __builtin_amdgcn_permlane16_swap(ml[0], ml[1], false, false);
      auto r3 = __builtin_amdgcn_permlane32_swap(ubits(fmx(fbits(r1[0]), fbits(r1[1]))), ubits(fmx(fbits(r2[0]), fbits(r2[1]))), false, false);
      const unsigned w = ubits(fmx(fbits(r3[0]), fbits(r3[1])) * -CS);
      auto r4 = __builtin_amdgcn_permlane32_swap(w, w, false, false);
      auto r5 = __builtin_amdgcn_permlane16_swap(r4[0], r4[0], false, false);
      auto r6 = __builtin_amdgcn_permlane16_swap(r4[1], r4[1], false, false);
      mc[0][2 * p] = fbits(r5[0]); mc[0][2 * p + 1] = fbits(r5[1]); mc[1][2 * p] = fbits(r6[0]); mc[1][2 * p + 1] = fbits(r6[1]);
    };
    auto weigh = [&](int h) AI {
#pragma unroll
      for (int t = 1; t >= 0; --t) {
        const f32x4 a = __builtin_elementwise_fma(s[t][h], f32x4{CS, CS, CS, CS}, f32x4{mc[t][h], mc[t][h], mc[t][h], mc[t][h]});
        unsigned pr[4];
#pragma unroll
        for (int j = 0; j < 4; ++j) pr[j] = ubits(__builtin_amdgcn_exp2f(a[j]));
        const bf16x8 pb = cat(u32x2{__builtin_amdgcn_perm(pr[1], pr[0], ac.selP[0]), __builtin_amdgcn_perm(pr[3], pr[2], ac.selP[1])},
                              u32x2{__builtin_amdgcn_perm(pr[1], pr[0], ac.selC[0]), __builtin_amdgcn_perm(pr[3], pr[2], ac.selC[1])});
        l[t][h] = mfma(ones, pb, z4);
        o[t][h] = mfma(t ? hi4(V[h]) : lo4(V[h]), pb, z4);
        e[t][h] = mfma(t ? hi4(V[4]) : lo4(V[4]), pb, z4);
      }
    };
    auto emit = [&](int p) AI {
#pragma unroll
      for (int t = 1; t >= 0; --t) {
#pragma unroll
        for (int h = 2 * p; h < 2 * p + 2; ++h) { rl[t][h] = __builtin_amdgcn_rcpf(l[t][h][0]); op[t][h] = pk4(o[t][h] * rl[t][h]); }
        put(2 * t + p, u32x4{op[t][2 * p][0], op[t][2 * p][1], op[t][2 * p + 1][0], op[t][2 * p + 1][1]});
      }
    };
    set2(V[0], 2, pk4(acc[0][10])); set2(V[1], 2, pk4(acc[0][11])); set2(V[4], 2, pk4(acc[0][14]));
    maxima(0);
    PIN(V[0]); PIN(V[1]); PIN(V[4]);
    weigh(0); weigh(1); SB();
    scores(1); emit(0); SB();
    set2(V[2], 2, pk4(acc[0][12])); set2(V[3], 2, pk4(acc[0][13]));
    maxima(1);
    PIN(V[2]); PIN(V[3]);
    u32x4 ex[2]; unsigned rx[2];
#pragma unroll
    for (int t = 1; t >= 0; --t) {
      ex[t] = (__builtin_bit_cast(u32x4, e[t][1]) & ac.gm[1]) | (__builtin_bit_cast(u32x4, e[t][0]) & ~ac.gm[1]);
      rx[t] = (ubits(rl[t][1]) & ac.gm[1]) | (ubits(rl[t][0]) & ~ac.gm[1]);
    }
    weigh(2); weigh(3); SB();
    if (wid == 2) {
      const float gt = rbf(__builtin_amdgcn_rcpf(1.f + __expf(-rbf(logit))));
      if (lane < 8)
        __hip_atomic_store((GAS u64*)(ws + WS_GATE + (tg * 128 + 8 * hg + lane) * 8), ((u64)(unsigned)mktag(epoch) << 32) | ubits(gt), __ATOMIC_RELAXED, __HIP_MEMORY_SCOPE_SYSTEM);
    }
    emit(1); SB();
    u32x2 ox[2];
#pragma unroll
    for (int t = 1; t >= 0; --t) {
#pragma unroll
      for (int h = 2; h < 4; ++h) { ex[t] = (__builtin_bit_cast(u32x4, e[t][h]) & ac.gm[h]) | (ex[t] & ~ac.gm[h]); rx[t] = (ubits(rl[t][h]) & ac.gm[h]) | (rx[t] & ~ac.gm[h]); }
      ox[t] = pk4(__builtin_bit_cast(f32x4, ex[t]) * fbits(rx[t]));
    }
    put(4, u32x4{ox[0][0], ox[0][1], ox[1][0], ox[1][1]});
    {
      const u32x4 c4 = {(unsigned)__builtin_amdgcn_mov_dpp((int)cs, 0x00, 0xf, 0xf, true), (unsigned)__builtin_amdgcn_mov_dpp((int)cs, 0x55, 0xf, 0xf, true),
                        (unsigned)__builtin_amdgcn_mov_dpp((int)cs, 0xaa, 0xf, 0xf, true), (unsigned)__builtin_amdgcn_mov_dpp((int)cs, 0xff, 0xf, 0xf, true)};
      if ((lane & 3) == 0) __builtin_amdgcn_raw_buffer_store_b128(c4, orsrc, lane * 4, obase + 5120, SYS);
    }
    STAMP(3);
    setup4();
    bar();
    pre_read();
    STAMP(10);
    asm volatile("s_waitcnt vmcnt(0)" ::: "memory");
    STAMP(11);
    bar_l();
    project(IC(0));
  }
  const u64 tag = mktag(epoch);
  if (__builtin_expect(__builtin_amdgcn_ballot_w64(((unsigned)(gv[0] >> 32) ^ (unsigned)tag) | ((unsigned)(gv[1] >> 32) ^ (unsigned)tag) | ((unsigned)(gv[2] >> 32) ^ (unsigned)tag)) != 0, 0)) {
#pragma unroll
    for (int e = 0; e < 3; ++e)
#pragma unroll 1
      for (int spin = 0; spin < SPIN; ++spin) { gv[e] = __hip_atomic_load((const GAS u64*)(ws + go[e]), RLX); if (__builtin_amdgcn_ballot_w64((unsigned)(gv[e] >> 32) != (unsigned)tag) == 0) break; }
  }
  char* const out = (char*)ptr_of(ka, 6);
  auto finish = [&](auto E) AI {
    constexpr int e = E.value;
    LAS char* pr = prd[e];
    const float gt = fbits((unsigned)gv[e]), gn = rbf(1.f - gt);
    const f32x4 lo = (*(LAS f32x4*)(pr) + *(LAS f32x4*)(pr + 10240)) * gt + (*(LAS f32x4*)(pr + 20480) + *(LAS f32x4*)(pr + 30720)) * gn;
    const f32x4 hi = (*(LAS f32x4*)(pr + 5120) + *(LAS f32x4*)(pr + 15360)) * gt + (*(LAS f32x4*)(pr + 25600) + *(LAS f32x4*)(pr + 35840)) * gn;
    const u32x2 l2 = pk4(lo), h2 = pk4(hi);
    __builtin_amdgcn_raw_buffer_store_b128(u32x4{l2[0], l2[1], h2[0], h2[1]}, rsrc(out, 2048 * 1280), oo[e], 0, SYS);
  };
  if (!th) {
    __builtin_amdgcn_s_setprio(0);
    bar_l();
    finish(IC(0));
    if (wid == 0) finish(IC(1));
  }
  bar_l();
  if (wid < 5) finish(IC(2));
  if (hg == 0 && tid == 0) __hip_atomic_store((GAS u64*)(ws + WS_E + tg * 128), tag, __ATOMIC_RELAXED, __HIP_MEMORY_SCOPE_SYSTEM);
  STAMP(8);
}

extern "C" void kernel_launch(void* const* d_in, const int* in_sizes, int n_in, void* d_out, int out_size, void* d_ws, size_t ws_size, hipStream_t stream) {
  (void)in_sizes; (void)n_in; (void)out_size; (void)ws_size;
  (void)hipFuncSetAttribute((const void*)fused_kernel, hipFuncAttributeMaxDynamicSharedMemorySize, LDS_BYTES);
  fused_kernel<<<256, 512, LDS_BYTES, stream>>>((const char*)d_in[0], (const char*)d_in[1], (const char*)d_in[4], (const char*)d_in[6], (const char*)d_in[2],
                                                (const char*)d_in[5], (char*)d_out, (char*)d_ws);
}
```

```cpp
#include <hip/hip_runtime.h>
#include <hip/hip_bf16.h>
#include <utility>

typedef __bf16 bf16x8 __attribute__((ext_vector_type(8)));
typedef __bf16 bf16x4 __attribute__((ext_vector_type(4)));
typedef __bf16 bf16x2 __attribute__((ext_vector_type(2)));
typedef float f32x4 __attribute__((ext_vector_type(4)));
typedef float f32x2 __attribute__((ext_vector_type(2)));
typedef unsigned u32x6 __attribute__((ext_vector_type(6)));
typedef unsigned u32x4 __attribute__((ext_vector_type(4)));
typedef unsigned u32x2 __attribute__((ext_vector_type(2)));
typedef unsigned long long u64;
#define LAS __attribute__((address_space(3)))
#define GAS __attribute__((address_space(1)))
#define DI __device__ __forceinline__
#define AI __attribute__((always_inline))
#define SB() __builtin_amdgcn_sched_barrier(0)
#define PIN(x) asm volatile("" : "+v"(x))
#define RLX __ATOMIC_RELAXED, __HIP_MEMORY_SCOPE_AGENT

constexpr int STG = 49152;
constexpr int XOFF = 30720;
constexpr int EXCH = 143360;
constexpr int GWL = EXCH + 3 * 5120;
constexpr int LDS_BYTES = 163840;
constexpr int WS_E = 0;
constexpr int WS_GATE = 4096;
constexpr int WS_O = 32768;
constexpr int REC = 5376;
constexpr int O_BYTES = 16 * 16 * 4 * REC;
constexpr int SPIN = 1 << 16;
constexpr int SYS = 17, DEV = 16;
constexpr unsigned CODE_BYTES = 28672;

DI void bar() { asm volatile("s_barrier" ::: "memory"); }
DI void bar_l() { __builtin_amdgcn_s_waitcnt(0xc07f); asm volatile("s_barrier" ::: "memory"); }
template <int N> DI void bar_v() { asm volatile("s_waitcnt vmcnt(%0)\n\ts_barrier" :: "i"(N) : "memory"); }
DI bf16x8 cat(bf16x4 a, bf16x4 b) { return __builtin_shufflevector(a, b, 0, 1, 2, 3, 4, 5, 6, 7); }
DI bf16x8 cat(u32x2 a, u32x2 b) { u32x4 r = {a[0], a[1], b[0], b[1]}; return __builtin_bit_cast(bf16x8, r); }
DI u32x6 cat3(u32x2 a, u32x2 b, u32x2 c) { u32x6 r = {a[0], a[1], b[0], b[1], c[0], c[1]}; return r; }
DI bf16x8 lo4(u32x6 t) { return __builtin_bit_cast(bf16x8, __builtin_shufflevector(t, t, 0, 1, 2, 3)); }
DI bf16x8 hi4(u32x6 t) { return __builtin_bit_cast(bf16x8, __builtin_shufflevector(t, t, 2, 3, 4, 5)); }
DI bf16x4 trd(LAS char* p) { return __builtin_amdgcn_ds_read_tr16_b64_v4bf16((LAS bf16x4*)p); }
DI unsigned pk2(float a, float b) { f32x2 v = {a, b}; return __builtin_bit_cast(unsigned, __builtin_convertvector(v, bf16x2)); }
DI u32x2 pk4(f32x4 v) { u32x2 r = {pk2(v[0], v[1]), pk2(v[2], v[3])}; return r; }
DI f32x4 mfma(bf16x8 a, bf16x8 b, f32x4 c) { return __builtin_amdgcn_mfma_f32_16x16x32_bf16(a, b, c, 0, 0, 0); }
DI float rbf(float v) { return __builtin_bit_cast(float, pk2(v, v) << 16); }
DI u64 mktag(u64 e) { return e * 6364136223846793005ull + 1442695040888963407ull; }
DI unsigned sum4(u32x4 v) { return v[0] + v[1] + v[2] + v[3]; }
DI float fmx(float a, float b) { return __builtin_elementwise_maximum(a, b); }
DI float fbits(unsigned u) { return __builtin_bit_cast(float, u); }
DI unsigned ubits(float f) { return __builtin_bit_cast(unsigned, f); }
template <class F, int... I> DI void sfor_(F&& f, std::integer_sequence<int, I...>) { (f(std::integral_constant<int, I>{}), ...); }
template <int N, class F> DI void sfor(F&& f) { sfor_(f, std::make_integer_sequence<int, N>{}); }
#define IC(v) std::integral_constant<int, (v)>{}

constexpr int CARRY = 4;
constexpr int PRE = 1;
constexpr int EGAP = 5;
constexpr int GATE_F = 4;
constexpr bool has_halo(int W, int f) { return f >= 5 && ((f - 5) & 3) == W; }
constexpr int grp_n(int W, int n) { return (has_halo(W, n % 15) || (W == 2 && n % 15 == GATE_F)) ? 3 : 2; }
constexpr int m_grp(int W, int m) { int n = 30 - CARRY; while (m >= grp_n(W, n % 30)) { m -= grp_n(W, n % 30); ++n; } return n - (30 - CARRY); }
constexpr int m_idx(int W, int m) { int n = 30 - CARRY; while (m >= grp_n(W, n % 30)) { m -= grp_n(W, n % 30); ++n; } return m; }
constexpr int n_mfma(int W) { int t = 0; for (int n = 0; n < 30; ++n) t += grp_n(W, n); return t; }
constexpr int n_read(int W) { return W == 2 ? 68 : 66; }
constexpr int rd_code(int W, int j) {
  constexpr int pos[2][10] = {{0, 3, 10, -1, -1, 29, 32, 41, -1, -1}, {0, 3, 8, -1, 11, 30, 33, 40, -1, 43}};
  int k = 0;
  for (int i = 0; i < 10; ++i) { const int p = pos[W == 2][i]; if (p < 0) continue; if (j == p) return 60 + i; if (j > p) ++k; }
  return j - k;
}

template <int W>
DI void gemm1(LAS char* lds, int lane, int hg, f32x4 (&acc)[2][15], float& logit) {
  const int g = lane >> 4, i16 = lane & 15, q = i16 >> 2, p = lane & 3;
  const unsigned rowb = (16 * (g >> 1) + 4 * (g & 1) + q) * 480;
  const unsigned aM = rowb + 8 * p, aX = rowb + 40 * p + 32;
  const unsigned aA = XOFF + i16 * 128 + ((g ^ (i16 >> 1)) * 16);
  const int rh = i16 < 8 ? 16 + 8 * hg + i16 : i16;
  const unsigned aH = XOFF + rh * 128 + ((g ^ ((rh >> 1) & 7)) * 16);
  f32x4 hal[3];
#pragma unroll
  for (int t = 0; t < 2; ++t)
#pragma unroll
    for (int n = 0; n < 15; ++n) acc[t][n] = f32x4{0, 0, 0, 0};
#pragma unroll
  for (int n = 0; n < 3; ++n) hal[n] = f32x4{0, 0, 0, 0};
  bf16x8 cw[CARRY], cx[3];
  const bf16x8 z8 = __builtin_bit_cast(bf16x8, u32x4{0, 0, 0, 0});
#pragma unroll
  for (int n = 0; n < CARRY; ++n) cw[n] = z8;
#pragma unroll
  for (int n = 0; n < 3; ++n) cx[n] = z8;
  auto one = [&](int f, int i, bf16x8 wf, bf16x8 xf) AI {
    f32x4& d = i < 2 ? acc[i][f] : hal[(f - 5) >> 2];
    d = f < 10 ? mfma(wf, xf, d) : mfma(xf, wf, d);
  };
  constexpr int NM = n_mfma(W), NR = n_read(W), NP = W == 2 ? 7 : 6;
  unsigned buf = 0;
  LAS char* pp[7] = {lds + aM, lds + aX, lds + aA, lds + (aA ^ 64), lds + aH, lds + (aH ^ 64), lds + (GWL + g * 16)};
  int it = 10, dl = STG;
#pragma unroll 1
  do {
    bf16x4 wh[60]; bf16x8 xf[10];
    auto rd = [&](auto J) AI {
      constexpr int c = rd_code(W, J.value);
      if constexpr (c < 60) {
        constexpr int n = c >> 1, ks = n / 15, f = n % 15, sec = f / 5, jj = f % 5;
        wh[c] = trd((jj < 4 ? pp[0] + 40 * jj : pp[1]) + (ks * 32 + (c & 1) * 8) * 480 + sec * 160);
      } else {
        constexpr int ks = (c - 60) / 5, t = (c - 60) % 5;
        xf[c - 60] = *(LAS bf16x8*)(t < 2 ? pp[2 + ks] + (2 * W + 1 + t) * 2048 : t == 2 ? pp[4 + ks] : pp[6] + ks * 64);
      }
    };
    sfor<NM>([&](auto M) AI {
      constexpr int m = M.value, gi = m_grp(W, m), i0 = m_idx(W, m), i = (i0 < 2 && (gi & 1)) ? 1 - i0 : i0;
      if constexpr (m == PRE) { bar_l(); SB(); }
      if constexpr (gi < CARRY) one(15 - CARRY + gi, i, cw[gi], cx[i]);
      else {
        constexpr int n = gi - CARRY, ks = n / 15;
        if constexpr (W == 2 && n % 15 == GATE_F && i == 2) hal[2] = mfma(xf[ks * 5 + 4], xf[ks * 5 + 2], hal[2]);
        else one(n % 15, i, cat(wh[2 * n], wh[2 * n + 1]), xf[ks * 5 + i]);
      }
      SB();
      constexpr int D2 = NR - (NM - EGAP), j = m - PRE;
      if constexpr (j >= 0 && j < D2) { rd(IC(2 * j)); rd(IC(2 * j + 1)); }
      else if constexpr (j >= 0 && D2 + j < NR) rd(IC(D2 + j));
      else if constexpr (j >= 0) {
        constexpr int u = m - (NM - (EGAP - PRE)), NG = EGAP - PRE, PER = (NP + NG - 1) / NG;
        sfor<PER>([&](auto K) AI { constexpr int k = u * PER + K.value; if constexpr (k < NP) { pp[k] += (k == 6 ? 128 : dl); PIN(pp[k]); } });
      }
      if constexpr (m == 21) { dl = (buf == 2 * STG) ? -2 * STG : STG; asm volatile("" : "+s"(dl)); }
      if constexpr (m == 23) { buf += dl; --it; asm volatile("" : "+s"(buf), "+s"(it)); }
      SB();
    });
#pragma unroll
    for (int n = 0; n < CARRY; ++n) cw[n] = cat(wh[2 * (30 - CARRY + n)], wh[2 * (30 - CARRY + n) + 1]);
#pragma unroll
    for (int n = 0; n < 3; ++n) cx[n] = xf[5 + n];
  } while (it);
#pragma unroll
  for (int gi = 0; gi < CARRY; ++gi)
#pragma unroll
    for (int i = 0; i < 3; ++i)
      if (i < grp_n(W, 30 - CARRY + gi)) one(15 - CARRY + gi, i, cw[gi], cx[i]);
#pragma unroll
  for (int f = 5; f < 15; ++f)
    if (has_halo(W, f)) *(LAS u32x2*)(lds + EXCH + (f - 5) * 512 + lane * 8) = pk4(hal[(f - 5) >> 2]);
  if (W == 2) logit = hal[2][0];
}

struct AttnConst { f32x4 biasC, biasP, biasP0; unsigned selC[2], selP[2], gm[4]; };
DI bf16x8 q_operand(int h, u32x2 qh, u32x2 qx, unsigned gm) { const u32x2 qm = {qx[0] & gm, qx[1] & gm}; return (h & 1) ? cat(qm, qh) : cat(qh, qm); }
DI bf16x8 k_operand(int h, const u32x6 (&k)[2]) { return (h & 1) ? hi4(k[h >> 1]) : lo4(k[h >> 1]); }
#define PW "s_waitcnt vmcnt(3)\n\t"
#define DMA(v, srd, so, mo, si, mi) "s_add_u32 m0, " mo ", " #mi "\n\ts_add_u32 %0, " so ", " #si "\n\tbuffer_load_dwordx4 " v ", " srd ", %0 offen lds\n\t"

typedef unsigned u32x16 __attribute__((ext_vector_type(16)));
DI const char* ptr_of(u32x16 k, int i) { return (const char*)(((u64)k[2 * i + 1] << 32) | k[2 * i]); }
DI __amdgpu_buffer_rsrc_t rsrc(const char* p, int bytes) { return __builtin_amdgcn_make_buffer_rsrc((void*)p, 0, bytes, 0x00020000); }

extern "C" __global__ __launch_bounds__(512, 2) void fused_kernel(const char*, const char*, const char*, const char*, const char*, const char*, char*, char*) {
  extern __shared__ __attribute__((aligned(16))) char smem[];
  u32x16 ka;
  asm volatile("s_load_dwordx16 %0, %1, 0x0" : "=&s"(ka) : "s"(__builtin_amdgcn_kernarg_segment_ptr()));
  LAS char* lds = (LAS char*)smem;
  const unsigned ldsb = (unsigned)(size_t)lds;
  const int tid = threadIdx.x, lane = tid & 63;
  const int wid = __builtin_amdgcn_readfirstlane(tid >> 6);
  const int b = blockIdx.x, xcd = b & 7, r = b >> 3;
  const int tg = (xcd & 1) * 8 + (r & 7), hg = (xcd >> 1) * 4 + (r >> 3);
  const int par = tg >> 3, ch = tg & 7;
  const int kq = wid & 3, th = wid >> 2;
  if (wid == 2) {
    const char* pc; unsigned back;
    asm volatile("s_getpc_b64 %0\nfk_here:\n\ts_mov_b32 %1, fk_here-fused_kernel" : "=s"(pc), "=s"(back));
    pc -= back;
    const unsigned po = (r * 8 + (lane & 7)) * 128;
    if (lane < 8 && po < CODE_BYTES) asm volatile("s_mov_b32 m0, %2\n\ts_nop 0\n\tglobal_load_lds_dword %0, %1" :: "v"(po), "s"(pc), "s"(ldsb + XOFF) : "memory");
  }
  const int stg0 = (((5 * ((ch + hg) & 3) + 1) >> 1) + par + ((hg >> 2) & 1)) % 10;
  const int l30 = lane >= 30 ? lane - 30 : lane, sec = (l30 >= 10) + (l30 >= 20);
  unsigned vW = (lane >= 30 ? 3840 : 0) + sec * (1280 - 160) + l30 * 16;
  unsigned vX0 = (lane >> 3) * 2560 + (((lane & 7) ^ (lane >> 4)) * 16), vX1 = vX0 ^ 64;
  unsigned vH = (lane >> 3) * 2560 + (((lane & 7) ^ (4 + kq)) * 16);
  unsigned oW = kq * 16 * 3840 + (hg & 7) * 160;
  unsigned oX = (2 * (128 * ch + 32 * kq) + par) * 1280;
  unsigned oH = (2 * (128 * ch + 2 * kq - (ch ? 8 : 0)) + par) * 1280;
  unsigned sW0 = oW + stg0 * (64 * 3840), sX0 = oX + stg0 * 128, sH0 = oH + stg0 * 128;
  unsigned dW = ldsb + kq * 7680, dX = ldsb + XOFF + (16 + 32 * kq) * 128, dH = ldsb + XOFF + (8 + 2 * kq) * 128;
  asm volatile("s_waitcnt lgkmcnt(0)" : "+s"(ka), "+v"(vW), "+v"(vX0), "+v"(vX1), "+v"(vH), "+s"(sW0), "+s"(sX0), "+s"(sH0), "+s"(dW), "+s"(dX), "+s"(dH));
  const char* const x = ptr_of(ka, 0);
  char* const ws = (char*)ptr_of(ka, 7);
  u64 epoch, epoch_in;
  asm volatile("s_load_dwordx2 %0, %1, 0x0" : "=&s"(epoch_in) : "s"(ws + WS_E + tg * 128) : "memory");

  const auto wsrd = rsrc(hg >= 8 ? ptr_of(ka, 2) : ptr_of(ka, 1), 640 * 3840), xsrd = rsrc(x, 2048 * 1280);
  auto partA = [&](unsigned sw_, unsigned dw) AI {
    unsigned t;
    asm volatile(
      "s_mov_b32 exec_hi, 0x0fffffff\n\t"
      DMA("%1", "%2", "%3", "%4", 0, 0) DMA("%1", "%2", "%3", "%4", 7680, 960) DMA("%1", "%2", "%3", "%4", 30720, 1920)
      DMA("%1", "%2", "%3", "%4", 38400, 2880) DMA("%1", "%2", "%3", "%4", 15360, 3840) DMA("%1", "%2", "%3", "%4", 23040, 4800)
      "s_mov_b32 exec_hi, -1"
      : "=&s"(t) : "v"(vW), "s"(wsrd), "s"(sw_), "s"(dw) : "scc", "memory");
  };
  auto partB = [&](unsigned sw_, unsigned sx, unsigned sh, unsigned dw, unsigned dx, unsigned dh) AI {
    unsigned t;
    asm volatile(
      DMA("%2", "%6", "%8", "%11", 0, 0) DMA("%3", "%6", "%8", "%11", 20480, 1024) DMA("%2", "%6", "%8", "%11", 40960, 2048) DMA("%3", "%6", "%8", "%11", 61440, 3072)
      "s_mov_b32 exec_hi, 0x0fffffff\n\t"
      DMA("%1", "%5", "%7", "%10", 46080, 5760) DMA("%1", "%5", "%7", "%10", 53760, 6720)
      "s_mov_b64 exec, 0xffff\n\t"
      DMA("%4", "%6", "%9", "%12", 0, 0)
      "s_mov_b64 exec, -1"
      : "=&s"(t)
      : "v"(vW), "v"(vX0), "v"(vX1), "v"(vH), "s"(wsrd), "s"(xsrd), "s"(sw_), "s"(sx), "s"(sh), "s"(dw), "s"(dx), "s"(dh)
      : "scc", "memory");
  };
  if (wid >= 4) {
    partB(sW0, sX0, sH0, dW, dX, dH); partA(sW0, dW);
    if (kq < 2) {
      const int c = kq * 64 + lane, sg = stg0 + (c >> 3);
      unsigned t;
      asm volatile(DMA("%1", "%2", "0", "%3", 0, 0) : "=&s"(t)
                   : "v"(c < 80 ? (sg >= 10 ? sg - 10 : sg) * 128 + (c & 7) * 16 : 2048), "s"(rsrc(ptr_of(ka, 3), 1280)), "s"(ldsb + GWL + kq * 1024) : "scc", "memory");
    }
    partA(stg0 == 9 ? oW : sW0 + 64 * 3840, dW + STG);
  }
  SB();
  const int g = lane >> 4, i16 = lane & 15;
  const int hf = (hg >> 2) & 1, ct = (hg & 3) + 4 * (hg >> 3);
  unsigned so[4][3];
  LAS char *pw, *pw3;
  auto setup3 = [&]() AI {
    int tq = tg; asm volatile("" : "+s"(tq));
#pragma unroll
    for (int i = 0; i < 4; ++i) {
      const unsigned ob = ((tq * 16 + 4 * kq + ((tq + i) & 3)) * 4 + 2 * hf + th) * REC;
      so[i][0] = ob + (tq & 4) * 256; so[i][1] = ob + ((tq & 4) ^ 4) * 256; so[i][2] = ob + 4096;
      asm volatile("" : "+s"(so[i][0]), "+s"(so[i][1]), "+s"(so[i][2]));
    }
  };
  unsigned oo[3], go[3]; LAS char *pwr, *prd[3];
  auto setup4 = [&]() AI {
    int td = tid; PIN(td);
#pragma unroll
    for (int e = 0; e < 3; ++e) {
      const int c = e == 1 ? 256 + (td & 63) : td < 320 ? td : 0, tk = c / 10, tok = tk + (e == 2 ? 32 : 0), j = c - tk * 10;
      oo[e] = (2 * (128 * ch + 64 * hf + tok) + par) * 1280 + ct * 160 + j * 16; PIN(oo[e]);
      go[e] = WS_GATE + (tg * 128 + 64 * hf + tok) * 8; PIN(go[e]);
      prd[e] = lds + ((e == 2 ? 0 : 122880) + (c ^ ((tk >> 2) & 1)) * 16); PIN(prd[e]);
    }
    pwr = lds + ((th ? 0 : 122880) + kq * 10240 + ((td >> 4) & 1) * 5120 + (td & 15) * 160 + (((td >> 5) ^ (td >> 2)) & 1) * 16);
    PIN(pwr);
  };
  bf16x8 pre[4][5];
  auto pre_read = [&]() AI {
    int ln = lane; PIN(ln);
    pw = lds + ((ln >> 2) * 160 + 8 * (ln & 3) + kq * 5120); pw3 = pw + 3 * 20480;
    PIN(pw); PIN(pw3);
#pragma unroll
    for (int ps = 0; ps < 4; ++ps)
#pragma unroll
      for (int c5 = 0; c5 < 5; ++c5) pre[ps][c5] = cat(trd(pw3 + ps * 20480 + c5 * 32), trd(pw3 + ps * 20480 + c5 * 32 + 2560));
  };
  u32x4 late[3][5];

  u64 gv[3];
  auto project = [&](auto THC) AI {
    constexpr bool TH = THC.value;
    const u64 tag = mktag(epoch);
    const auto orsrc = rsrc(ws + WS_O, O_BYTES);
    struct Rec { u32x4 m[2][2], x; unsigned cs; };
    Rec rec[4];
    const unsigned l16 = lane * 16, l4 = lane * 4;
    auto ldrec = [&](int i, int j) AI {
      if (j < 4) rec[i].m[j >> 1][j & 1] = __builtin_amdgcn_raw_buffer_load_b128(orsrc, l16 + (j & 1) * 2048, so[i][j >> 1], DEV);
      else if (j == 4) rec[i].x = __builtin_amdgcn_raw_buffer_load_b128(orsrc, l16, so[i][2], DEV);
      else rec[i].cs = __builtin_amdgcn_raw_buffer_load_b32(orsrc, l4 + 1024, so[i][2], DEV);
    };
    auto reload = [&](int i) AI {
      Rec& q = rec[i];
      asm volatile("buffer_load_dwordx4 %0, %6, %8, %9 offen sc1\n\tbuffer_load_dwordx4 %1, %6, %8, %9 offen offset:2048 sc1\n\t"
                   "buffer_load_dwordx4 %2, %6, %8, %10 offen sc1\n\tbuffer_load_dwordx4 %3, %6, %8, %10 offen offset:2048 sc1\n\t"
                   "buffer_load_dwordx4 %4, %6, %8, %11 offen sc1\n\tbuffer_load_dword %5, %7, %8, %11 offen offset:1024 sc1\n\ts_waitcnt vmcnt(0)"
                   : "+v"(q.m[0][0]), "+v"(q.m[0][1]), "+v"(q.m[1][0]), "+v"(q.m[1][1]), "+v"(q.x), "+v"(q.cs)
                   : "v"(l16), "v"(l4), "s"(orsrc), "s"(so[i][0]), "s"(so[i][1]), "s"(so[i][2]) : "memory");
    };
    auto word = [&](int i, int k) AI { return k < 16 ? rec[i].m[k >> 3][(k >> 2) & 1][k & 3] : rec[i].x[k & 3]; };
    auto stale = [&](int i) AI {
      unsigned c = (unsigned)tag;
#pragma unroll
      for (int k = 0; k < 20; ++k) c += word(i, k);
      return __builtin_amdgcn_ballot_w64(c != rec[i].cs);
    };
    auto refetch = [&](int i, int n) AI {
#pragma unroll 1
      for (int spin = 0; spin < SPIN; ++spin) { reload(i); if (!stale(i)) break; }
#pragma unroll
      for (int k = 1; k < n; ++k) reload(i + k);
    };
    __builtin_amdgcn_s_setprio(TH ? 1 : 2);
    if constexpr (TH) {
      LAS char* lp = lds + 3 * 20480 + kq * 5120 + lane * 16;
#pragma unroll
      for (int ps = 0; ps < 3; ++ps)
#pragma unroll
        for (int i = 0; i < 5; ++i) *(LAS u32x4*)(lp + ((ps + 2) % 3) * 20480 + i * 1024) = late[(ps + 2) % 3][i];
      SB(); setup3(); setup4();
      bar_l();
    }
    SB();
    sfor<12>([&](auto J) AI { ldrec(J.value / 6, J.value % 6); SB(); });
    SB();
    bf16x4 wq[10][10];
    auto rdq = [&](auto P, auto I) AI {
      constexpr int G[10] = {3, 4, 5, 6, 0, 1, 2, 3, 4, 5};
      wq[P.value][I.value] = trd((G[P.value] < 3 ? pw + G[P.value] * 20480 : pw3 + (G[P.value] - 3) * 20480) + (I.value >> 1) * 32 + (I.value & 1) * 2560);
    };
    SB();
    if (__builtin_expect(stale(0) != 0, 0)) refetch(0, 2);
    if constexpr (TH) { if (__builtin_expect(stale(1) != 0, 0)) refetch(1, 1); }
    f32x4 a3[2][5];
    unsigned csum = 0; u64 bad = 0; bf16x8 xo[2];
    sfor<100>([&](auto M) AI {
      constexpr int m = M.value;
      constexpr int pos = m < 70 ? m / 10 : 7 + ((m - 70) % 6) / 2, c5 = m < 70 ? (m % 10) >> 1 : (m - 70) / 6, t = m & 1;
      constexpr int pp = pos < 4 ? pos : pos - 1;
      if constexpr ((m == 20 && !TH) || m == 50 || m == 70) { constexpr int i = m == 20 ? 1 : m == 50 ? 2 : 3; if (__builtin_expect(bad != 0, 0)) refetch(i, i == 1 ? 1 : 4 - i); SB(); }
      if constexpr (m == 80 && TH) { bar(); SB(); }
      const bf16x8 ov = (pos == 4 || pos == 9) ? xo[t] : __builtin_bit_cast(bf16x8, rec[pp >> 1].m[pp & 1][t]);
      const bf16x8 wv = pos < 4 ? pre[pos & 3][c5] : cat(wq[pos][2 * c5], wq[pos][2 * c5 + 1]);
      if constexpr (m < 10) asm volatile("v_mfma_f32_16x16x32_bf16 %0, %1, %2, 0" : "=&v"(a3[t][c5]) : "v"(wv), "v"(ov));
      else asm volatile("v_mfma_f32_16x16x32_bf16 %0, %1, %2, %0" : "+v"(a3[t][c5]) : "v"(wv), "v"(ov));
      SB();
      if constexpr (m < 12) ldrec(2 + m / 6, m % 6);
      if constexpr (!TH && m == 12) bar();
      if constexpr (!TH && m > 12 && m < 16) gv[m - 13] = __hip_atomic_load((const GAS u64*)(ws + go[m - 13]), RLX);
      if constexpr (TH && m == 12) { gv[2] = __hip_atomic_load((const GAS u64*)(ws + go[2]), RLX); gv[0] = gv[1] = gv[2]; }
      if constexpr (m >= 20 && m < 50) rdq(IC(m / 10 + 2), IC(m % 10));
      if constexpr (m >= 50 && m < 80) rdq(IC(7 + ((m - 50) % 6) / 2), IC(2 * ((m - 50) / 6) + (m & 1)));
      constexpr int ci = m < 20 ? 1 : m < 50 ? 2 : 3, cb = m < 20 ? 8 : m < 50 ? 36 : 56, k = m - cb;
      if constexpr (m < 70 && (m >= 20 || !TH) && k >= 0 && k < 10) { csum = (k ? csum : (unsigned)tag) + word(ci, 2 * k) + word(ci, 2 * k + 1); PIN(csum); }
      if constexpr (m < 70 && (m >= 20 || !TH) && k == 10) { bad = __builtin_amdgcn_ballot_w64(csum != rec[ci].cs); asm volatile("" : "+s"(bad)); }
      if constexpr (m == 30 || m == 31 || m == 70 || m == 71) {
        constexpr int r0 = m < 70 ? 0 : 2;
        xo[t] = cat(u32x2{rec[r0].x[2 * t], rec[r0].x[2 * t + 1]}, u32x2{rec[r0 + 1].x[2 * t], rec[r0 + 1].x[2 * t + 1]}); PIN(xo[t]);
      }
      if constexpr (m >= 82 && (m - 82) % 6 < 2) *(LAS f32x4*)(pwr + t * 16 * 160 + ((m - 82) / 6) * 32) = a3[t][(m - 82) / 6];
      SB();
    });
    *(LAS f32x4*)(pwr + 3 * 32) = a3[0][3];
    asm volatile("s_nop 1" : "+v"(a3[1][3]));
    *(LAS f32x4*)(pwr + 16 * 160 + 3 * 32) = a3[1][3];
    asm volatile("s_nop 5" : "+v"(a3[0][4]), "+v"(a3[1][4]));
    *(LAS f32x4*)(pwr + 4 * 32) = a3[0][4];
    *(LAS f32x4*)(pwr + 16 * 160 + 4 * 32) = a3[1][4];
  };

  if (__builtin_expect(wid >= 4, 0)) {
    int s1 = stg0 == 9 ? 0 : stg0 + 1, s2 = stg0 >= 8 ? stg0 - 8 : stg0 + 2;
    const auto wosrd = rsrc(kq >> 1 ? ptr_of(ka, 5) : ptr_of(ka, 4), 640 * 1280);
    const int rot = tg & 3, sw = (tg >> 2) & 1;
    unsigned vm[5], ve[5];
#pragma unroll
    for (int i = 0; i < 5; ++i) {
      const int c = i * 64 + lane, rr = c / 10, wi = c - rr * 10;
      vm[i] = (20 * (rr >> 4) + (rr & 15)) * 1280 + wi * 16; PIN(vm[i]);
      ve[i] = (20 * ((rr >> 2) & 3) + 16 + (rr & 3)) * 1280 + wi * 16; PIN(ve[i]);
    }
    auto wo_off = [&](int pos, unsigned (&v)[5], unsigned& sq) AI {
      if (pos == 4 || pos == 9) {
        const unsigned ha = (4 * (kq & 1) + ((rot + (pos == 9 ? 2 : 0)) & 3)) * 102400, hb = (4 * (kq & 1) + ((rot + (pos == 9 ? 3 : 1)) & 3)) * 102400;
#pragma unroll
        for (int i = 0; i < 5; ++i) v[i] = ve[i] + ((i * 64 + lane >= 160) ? hb : ha);
        sq = ct * 160;
      } else {
        const int pp = pos < 4 ? pos : pos - 1;
#pragma unroll
        for (int i = 0; i < 5; ++i) v[i] = vm[i];
        sq = ct * 160 + (4 * (kq & 1) + ((rot + (pp >> 1)) & 3)) * 102400 + ((pp & 1) ^ sw) * 51200;
      }
    };
    auto wo_pos = [&](int pos, int grp, bool paced) AI {
      unsigned v[5], sq, t;
      wo_off(pos, v, sq);
#define WO5(P) asm volatile(P DMA("%1", "%6", "%7", "%8", 0, 0) P DMA("%2", "%6", "%7", "%8", 0, 1024) P DMA("%3", "%6", "%7", "%8", 0, 2048) P DMA("%4", "%6", "%7", "%8", 0, 3072) P DMA("%5", "%6", "%7", "%8", 0, 4096) \
                   : "=&s"(t) : "v"(v[0]), "v"(v[1]), "v"(v[2]), "v"(v[3]), "v"(v[4]), "s"(wosrd), "s"(sq), "s"(ldsb + grp * 20480 + kq * 5120) : "scc", "memory")
      if (paced) WO5(PW); else WO5("");
    };
    auto late_load = [&](int ps, bool paced) AI {
      unsigned v[5], sq;
      wo_off(7 + ps, v, sq);
#pragma unroll
      for (int i = 0; i < 5; ++i) { if (paced) asm volatile(PW ::: "memory"); late[ps][i] = __builtin_amdgcn_raw_buffer_load_b128(wosrd, v[i], sq, 0); }
    };
    unsigned b1 = STG, b2 = 2 * STG;
    unsigned q0, q1, q2, q3, q4, q5, q6, q7;
    auto prep = [&]() AI {
      q0 = oW + s1 * (64 * 3840); q1 = oX + s1 * 128; q2 = oH + s1 * 128; q3 = dW + b1; q4 = dX + b1; q5 = dH + b1; q6 = oW + s2 * (64 * 3840); q7 = dW + b2;
      asm volatile("" : "+s"(q0), "+s"(q1), "+s"(q2), "+s"(q3), "+s"(q4), "+s"(q5), "+s"(q6), "+s"(q7));
      s1 = s2; s2 = (s2 == 9) ? 0 : s2 + 1;
      b1 = b2; b2 = (b2 == 2 * STG) ? 0 : b2 + STG;
    };
    prep();
    asm volatile("s_waitcnt vmcnt(6) lgkmcnt(0)\n\ts_mov_b64 %0, %1" : "=s"(epoch) : "s"(epoch_in) : "memory");
#pragma unroll 1
    for (int it = 0; it < 8; ++it) {
      SB(); bar(); SB();
      partB(q0, q1, q2, q3, q4, q5); partA(q6, q7);
      SB();
      prep();
      asm volatile("s_waitcnt vmcnt(6)" ::: "memory");
    }
    bar();
    partB(q0, q1, q2, q3, q4, q5);
    wo_pos(0, 3, false);
    bar_v<5>();
    wo_pos(1, 4, false); wo_pos(2, 5, false); wo_pos(3, 6, false);
    late_load(2, false);
    bar();
    wo_pos(4, 0, false); wo_pos(5, 1, true); wo_pos(6, 2, true);
    late_load(0, true);
    bar();
    late_load(1, false);
    pre_read();
    __builtin_amdgcn_s_waitcnt(0xc07f);
    bar_v<10>();
    project(IC(1));
  } else {
    const float NINF = -__builtin_inff();
    AttnConst ac;
    const bool first = (ch == 0) && (wid == 0);
#pragma unroll
    for (int j = 0; j < 4; ++j) {
      const int c = 4 * g + j;
      ac.biasC[j] = (c <= i16 && c >= i16 - 7) ? 0.f : NINF;
      ac.biasP[j] = (c >= i16 + 9) ? 0.f : NINF;
      ac.biasP0[j] = first ? NINF : ac.biasP[j];
      ac.gm[j] = (g == j) ? 0xffffffffu : 0u; PIN(ac.gm[j]);
    }
#pragma unroll
    for (int j = 0; j < 2; ++j) {
      const bool c0 = 4 * g + 2 * j <= i16, c1 = 4 * g + 2 * j + 1 <= i16;
      ac.selC[j] = (c0 ? 0x0302u : 0x0c0cu) | (c1 ? 0x07060000u : 0x0c0c0000u); PIN(ac.selC[j]);
      ac.selP[j] = (c0 ? 0x0c0cu : 0x0302u) | (c1 ? 0x0c0c0000u : 0x07060000u); PIN(ac.selP[j]);
    }
    PIN(ac.biasC); PIN(ac.biasP); PIN(ac.biasP0);
    setup3();
    f32x4 acc[2][15]; float logit = 0.f;
    asm volatile("s_waitcnt lgkmcnt(0)\n\ts_mov_b64 %0, %1" : "=s"(epoch) : "s"(epoch_in) : "memory");
    if (wid == 0) gemm1<0>(lds, lane, hg, acc, logit);
    else if (wid == 1) gemm1<1>(lds, lane, hg, acc, logit);
    else if (wid == 2) gemm1<2>(lds, lane, hg, acc, logit);
    else gemm1<3>(lds, lane, hg, acc, logit);
    asm volatile("s_waitcnt vmcnt(0)" ::: "memory");
    const auto orsrc = rsrc(ws + WS_O, O_BYTES);
    u32x6 K[3][2], V[5];
    auto set2 = [&](u32x6& t, int i, u32x2 v) AI { t[i] = v[0]; t[i + 1] = v[1]; };
    K[2][0] = cat3(pk4(acc[1][5]), pk4(acc[1][9]), pk4(acc[1][6])); PIN(K[2][0]);
    K[2][1] = cat3(pk4(acc[1][7]), u32x2{K[2][0][2], K[2][0][3]}, pk4(acc[1][8])); PIN(K[2][1]);
#pragma unroll
    for (int f = 0; f < 5; ++f) { asm volatile("" : "=v"(V[f])); set2(V[f], 4, pk4(acc[1][10 + f])); PIN(V[f]); }
    if (wid < 3) {
      LAS char* e = lds + EXCH + (wid + 1) * 5120 + lane * 8;
      *(LAS u32x2*)(e) = u32x2{K[2][0][0], K[2][0][1]}; *(LAS u32x2*)(e + 512) = u32x2{K[2][0][4], K[2][0][5]};
      *(LAS u32x2*)(e + 1024) = u32x2{K[2][1][0], K[2][1][1]}; *(LAS u32x2*)(e + 1536) = u32x2{K[2][1][4], K[2][1][5]};
      *(LAS u32x2*)(e + 2048) = u32x2{K[2][0][2], K[2][0][3]};
#pragma unroll
      for (int f = 0; f < 5; ++f) *(LAS u32x2*)(e + (5 + f) * 512) = u32x2{V[f][4], V[f][5]};
    }
    SB();
    const u32x2 qx[2] = {pk4(acc[0][4]), pk4(acc[1][4])};
    bf16x8 qb[2][4]; f32x4 sc[2][4], sp[2][4];
    auto scores = [&](int p) AI {
      K[1][p] = cat3(pk4(acc[0][5 + 2 * p]), p ? u32x2{K[1][0][2], K[1][0][3]} : pk4(acc[0][9]), pk4(acc[0][6 + 2 * p])); PIN(K[1][p]);
#pragma unroll
      for (int t = 1; t >= 0; --t)
#pragma unroll
        for (int h = 2 * p; h < 2 * p + 2; ++h) {
          qb[t][h] = q_operand(h, pk4(acc[t][h]), qx[t], ac.gm[h]);
          sc[t][h] = mfma(k_operand(h, K[t + 1]), qb[t][h], ac.biasC);
          if (t) sp[t][h] = mfma(k_operand(h, K[t]), qb[t][h], ac.biasP);
        }
    };
    scores(0);
    SB();
    bar_l();
    SB();
    {
      LAS char* e = lds + EXCH + wid * 5120 + lane * 8;
      K[0][0] = cat3(*(volatile LAS u32x2*)(e), *(volatile LAS u32x2*)(e + 2048), *(volatile LAS u32x2*)(e + 512));
      set2(V[0], 0, *(volatile LAS u32x2*)(e + 5 * 512)); set2(V[1], 0, *(volatile LAS u32x2*)(e + 6 * 512)); set2(V[4], 0, *(volatile LAS u32x2*)(e + 9 * 512));
      K[0][1] = cat3(*(volatile LAS u32x2*)(e + 1024), *(volatile LAS u32x2*)(e + 2048), *(volatile LAS u32x2*)(e + 1536));
      set2(V[2], 0, *(volatile LAS u32x2*)(e + 7 * 512)); set2(V[3], 0, *(volatile LAS u32x2*)(e + 8 * 512));
    }
    const unsigned obase = ((tg * 16 + hg) * 4 + wid) * REC;
    unsigned cs = (unsigned)mktag(epoch);
    auto put = [&](int k, u32x4 v) AI {
      __builtin_amdgcn_raw_buffer_store_b128(v, orsrc, lane * 16 + (k & 3) * 1024, obase + (k >> 2) * 4096, SYS);
      cs += sum4(v);
    };
    const float CS = 0.22360679774997896f * 1.4426950408889634f;
    const bf16x8 ones = __builtin_bit_cast(bf16x8, u32x4{0x3f803f80u, 0x3f803f80u, 0x3f803f80u, 0x3f803f80u});
    const f32x4 z4 = {0, 0, 0, 0};
    f32x4 s[2][4], l[2][4], o[2][4], e[2][4]; float mc[2][4], rl[2][4]; u32x2 op[2][4];
    auto maxima = [&](int p) AI {
      unsigned ml[4];
#pragma unroll
      for (int h = 2 * p; h < 2 * p + 2; ++h) { if (h == 2 * p) PIN(K[0][p]); sp[0][h] = mfma(k_operand(h, K[0]), qb[0][h], ac.biasP0); }
#pragma unroll
      for (int u = 3; u >= 0; --u) {
        const int t = u >> 1, h = 2 * p + (u & 1);
        s[t][h] = __builtin_elementwise_maximum(sc[t][h], sp[t][h]);
        ml[u] = ubits(fmx(fmx(fmx(s[t][h][0], s[t][h][1]), s[t][h][2]), s[t][h][3]));
      }
      auto r2 = __builtin_amdgcn_permlane16_swap(ml[2], ml[3], false, false);
      auto r1 = __builtin_amdgcn_permlane16_swap(ml[0], ml[1], false, false);
      auto r3 = __builtin_amdgcn_permlane32_swap(ubits(fmx(fbits(r1[0]), fbits(r1[1]))), ubits(fmx(fbits(r2[0]), fbits(r2[1]))), false, false);
      const unsigned w = ubits(fmx(fbits(r3[0]), fbits(r3[1])) * -CS);
      auto r4 = __builtin_amdgcn_permlane32_swap(w, w, false, false);
      auto r5 = __builtin_amdgcn_permlane16_swap(r4[0], r4[0], false, false);
      auto r6 = __builtin_amdgcn_permlane16_swap(r4[1], r4[1], false, false);
      mc[0][2 * p] = fbits(r5[0]); mc[0][2 * p + 1] = fbits(r5[1]); mc[1][2 * p] = fbits(r6[0]); mc[1][2 * p + 1] = fbits(r6[1]);
    };
    auto weigh = [&](int h) AI {
#pragma unroll
      for (int t = 1; t >= 0; --t) {
        const f32x4 a = __builtin_elementwise_fma(s[t][h], f32x4{CS, CS, CS, CS}, f32x4{mc[t][h], mc[t][h], mc[t][h], mc[t][h]});
        unsigned pr[4];
#pragma unroll
        for (int j = 0; j < 4; ++j) pr[j] = ubits(__builtin_amdgcn_exp2f(a[j]));
        const bf16x8 pb = cat(u32x2{__builtin_amdgcn_perm(pr[1], pr[0], ac.selP[0]), __builtin_amdgcn_perm(pr[3], pr[2], ac.selP[1])},
                              u32x2{__builtin_amdgcn_perm(pr[1], pr[0], ac.selC[0]), __builtin_amdgcn_perm(pr[3], pr[2], ac.selC[1])});
        l[t][h] = mfma(ones, pb, z4);
        o[t][h] = mfma(t ? hi4(V[h]) : lo4(V[h]), pb, z4);
        e[t][h] = mfma(t ? hi4(V[4]) : lo4(V[4]), pb, z4);
      }
    };
    auto emit = [&](int p) AI {
#pragma unroll
      for (int t = 1; t >= 0; --t) {
#pragma unroll
        for (int h = 2 * p; h < 2 * p + 2; ++h) { rl[t][h] = __builtin_amdgcn_rcpf(l[t][h][0]); op[t][h] = pk4(o[t][h] * rl[t][h]); }
        put(2 * t + p, u32x4{op[t][2 * p][0], op[t][2 * p][1], op[t][2 * p + 1][0], op[t][2 * p + 1][1]});
      }
    };
    set2(V[0], 2, pk4(acc[0][10])); set2(V[1], 2, pk4(acc[0][11])); set2(V[4], 2, pk4(acc[0][14]));
    maxima(0);
    PIN(V[0]); PIN(V[1]); PIN(V[4]);
    weigh(0); weigh(1); SB();
    scores(1); emit(0); SB();
    set2(V[2], 2, pk4(acc[0][12])); set2(V[3], 2, pk4(acc[0][13]));
    maxima(1);
    PIN(V[2]); PIN(V[3]);
    u32x4 ex[2]; unsigned rx[2];
#pragma unroll
    for (int t = 1; t >= 0; --t) {
      ex[t] = (__builtin_bit_cast(u32x4, e[t][1]) & ac.gm[1]) | (__builtin_bit_cast(u32x4, e[t][0]) & ~ac.gm[1]);
      rx[t] = (ubits(rl[t][1]) & ac.gm[1]) | (ubits(rl[t][0]) & ~ac.gm[1]);
    }
    weigh(2); weigh(3); SB();
    if (wid == 2) {
      const float gt = rbf(__builtin_amdgcn_rcpf(1.f + __expf(-rbf(logit))));
      if (lane < 8)
        __hip_atomic_store((GAS u64*)(ws + WS_GATE + (tg * 128 + 8 * hg + lane) * 8), ((u64)(unsigned)mktag(epoch) << 32) | ubits(gt), __ATOMIC_RELAXED, __HIP_MEMORY_SCOPE_SYSTEM);
    }
    emit(1); SB();
    u32x2 ox[2];
#pragma unroll
    for (int t = 1; t >= 0; --t) {
#pragma unroll
      for (int h = 2; h < 4; ++h) { ex[t] = (__builtin_bit_cast(u32x4, e[t][h]) & ac.gm[h]) | (ex[t] & ~ac.gm[h]); rx[t] = (ubits(rl[t][h]) & ac.gm[h]) | (rx[t] & ~ac.gm[h]); }
      ox[t] = pk4(__builtin_bit_cast(f32x4, ex[t]) * fbits(rx[t]));
    }
    put(4, u32x4{ox[0][0], ox[0][1], ox[1][0], ox[1][1]});
    {
      const u32x4 c4 = {(unsigned)__builtin_amdgcn_mov_dpp((int)cs, 0x00, 0xf, 0xf, true), (unsigned)__builtin_amdgcn_mov_dpp((int)cs, 0x55, 0xf, 0xf, true),
                        (unsigned)__builtin_amdgcn_mov_dpp((int)cs, 0xaa, 0xf, 0xf, true), (unsigned)__builtin_amdgcn_mov_dpp((int)cs, 0xff, 0xf, 0xf, true)};
      if ((lane & 3) == 0) __builtin_amdgcn_raw_buffer_store_b128(c4, orsrc, lane * 4, obase + 5120, SYS);
    }
    setup4();
    bar();
    pre_read();
    asm volatile("s_waitcnt vmcnt(0)" ::: "memory");
    bar_l();
    project(IC(0));
  }
  const u64 tag = mktag(epoch);
  if (__builtin_expect(__builtin_amdgcn_ballot_w64(((unsigned)(gv[0] >> 32) ^ (unsigned)tag) | ((unsigned)(gv[1] >> 32) ^ (unsigned)tag) | ((unsigned)(gv[2] >> 32) ^ (unsigned)tag)) != 0, 0)) {
#pragma unroll
    for (int e = 0; e < 3; ++e)
#pragma unroll 1
      for (int spin = 0; spin < SPIN; ++spin) { gv[e] = __hip_atomic_load((const GAS u64*)(ws + go[e]), RLX); if (__builtin_amdgcn_ballot_w64((unsigned)(gv[e] >> 32) != (unsigned)tag) == 0) break; }
  }
  char* const out = (char*)ptr_of(ka, 6);
  auto finish = [&](auto E) AI {
    constexpr int e = E.value;
    LAS char* pr = prd[e];
    const float gt = fbits((unsigned)gv[e]), gn = rbf(1.f - gt);
    const f32x4 lo = (*(LAS f32x4*)(pr) + *(LAS f32x4*)(pr + 10240)) * gt + (*(LAS f32x4*)(pr + 20480) + *(LAS f32x4*)(pr + 30720)) * gn;
    const f32x4 hi = (*(LAS f32x4*)(pr + 5120) + *(LAS f32x4*)(pr + 15360)) * gt + (*(LAS f32x4*)(pr + 25600) + *(LAS f32x4*)(pr + 35840)) * gn;
    const u32x2 l2 = pk4(lo), h2 = pk4(hi);
    __builtin_amdgcn_raw_buffer_store_b128(u32x4{l2[0], l2[1], h2[0], h2[1]}, rsrc(out, 2048 * 1280), oo[e], 0, SYS);
  };
  if (!th) {
    __builtin_amdgcn_s_setprio(0);
    bar_l();
    finish(IC(0));
    if (wid == 0) finish(IC(1));
  }
  bar_l();
  if (wid < 5) finish(IC(2));
  if (hg == 0 && tid == 0) __hip_atomic_store((GAS u64*)(ws + WS_E + tg * 128), tag, __ATOMIC_RELAXED, __HIP_MEMORY_SCOPE_SYSTEM);
}

extern "C" void kernel_launch(void* const* d_in, const int* in_sizes, int n_in, void* d_out, int out_size, void* d_ws, size_t ws_size, hipStream_t stream) {
  (void)in_sizes; (void)n_in; (void)out_size; (void)ws_size;
  (void)hipFuncSetAttribute((const void*)fused_kernel, hipFuncAttributeMaxDynamicSharedMemorySize, LDS_BYTES);
  fused_kernel<<<256, 512, LDS_BYTES, stream>>>((const char*)d_in[0], (const char*)d_in[1], (const char*)d_in[4], (const char*)d_in[6], (const char*)d_in[2],
                                                (const char*)d_in[5], (char*)d_out, (char*)d_ws);
}
```
